```python
import torch

def _softmax(x, axis=-1):
    return torch.softmax(x, dim=axis)
def _gelu(x):
    return torch.nn.functional.gelu(x, approximate="tanh")
def _silu(x):
    return torch.nn.functional.silu(x)
def _sigmoid(x):
    return torch.sigmoid(x)
def _layernorm(x, axis=-1, eps=1e-5):
    m = x.mean(dim=axis, keepdim=True)
    v = x.var(dim=axis, keepdim=True, unbiased=False)
    return (x - m) / torch.sqrt(v + eps)
def _scatter_add(tgt, idx, src, axis=0):
    return tgt.clone().index_add_(axis, idx, src)
def _segment_sum(src, seg, n):
    out = torch.zeros((n,) + src.shape[1:], dtype=src.dtype)
    return out.index_add_(0, seg, src)
def _pad(x, pw):
    flat = tuple(v for pair in reversed(pw) for v in pair)
    return torch.nn.functional.pad(x, flat)

kernel_name = 'gather_scatter__cheap_equivariant_egnn__graph'

def setup_inputs():

    _w = (lambda a: (lambda t: t.to(torch.bfloat16) if t.is_floating_point() else t)(torch.as_tensor(a)))
    N, deg, D, De = 10000, 64, 64, max(4, 1)
    E = N * deg
    return {
        "h": _w((lambda *s: torch.randn(*s, dtype=torch.bfloat16) * 0.2)(N, D)),
        "pos": _w((lambda *s: torch.randn(*s, dtype=torch.bfloat16) * 0.8)(N, 3)),
        "e_attr": _w((lambda *s: torch.randn(*s, dtype=torch.bfloat16) * 0.8)(E, De)),
        "src": _w((lambda lo, hi, *s: torch.randint(lo, hi, s))(0, N, E).to(torch.int64)),
        "dst": _w((lambda lo, hi, *s: torch.randint(lo, hi, s))(0, N, E).to(torch.int64)),
        "w_e": _w(0.1 * (lambda *s: torch.randn(*s, dtype=torch.bfloat16) * 0.8)(2 * D + 1 + De, D)),
        "w_x": _w(0.1 * (lambda *s: torch.randn(*s, dtype=torch.bfloat16) * 0.2)(D, 1)),
        "w_h": _w(0.1 * (lambda *s: torch.randn(*s, dtype=torch.bfloat16) * 0.2)(2 * D, D)),
    }

def reference(h, pos, e_attr, src, dst, w_e, w_x, w_h):
    N, D = h.shape[0], h.shape[1]
    diff = pos[dst] - pos[src]
    dist2 = torch.sum(diff * diff, axis=-1, keepdims=True)
    e_in = torch.concatenate([h[dst], h[src], dist2, e_attr], axis=-1)
    m_ij = _silu(e_in @ w_e)
    coord_scale = m_ij @ w_x
    pos_new = pos + _segment_sum(diff * coord_scale, dst, N)
    m_i = _segment_sum(m_ij, dst, N)
    h_new = h + _silu(torch.concatenate([h, m_i], axis=-1) @ w_h)
    return h_new, pos_new
```

```cpp
#include <hip/hip_runtime.h>
#include <hip/hip_bf16.h>
#include <stdint.h>

__device__ __forceinline__ float bf2f(uint16_t v){ return __uint_as_float(((uint32_t)v)<<16); }
__device__ __forceinline__ uint16_t f2bf(float f){ uint32_t u=__float_as_uint(f); u += 0x7fffu + ((u>>16)&1u); return (uint16_t)(u>>16); }
__device__ __forceinline__ float rbf(float f){ return bf2f(f2bf(f)); }

__global__ void k_zero(float* acc, int n){ int i=blockIdx.x*blockDim.x+threadIdx.x; if(i<n) acc[i]=0.f; }

__global__ void k_edge(const uint16_t* h,const uint16_t* pos,const uint16_t* ea,const int* src,const int* dst,
                       const uint16_t* we,const uint16_t* wx, float* macc, float* pacc, int E){
  int lane=threadIdx.x&63; int w=(blockIdx.x*blockDim.x+threadIdx.x)>>6; int nw=(gridDim.x*blockDim.x)>>6;
  for(int e=w;e<E;e+=nw){
    int s=src[e], d=dst[e];
    float df[3]; float d2=0;
    for(int k=0;k<3;k++){ df[k]=rbf(bf2f(pos[d*3+k])-bf2f(pos[s*3+k])); d2+=rbf(df[k]*df[k]); }
    d2=rbf(d2);
    float a=0;
    for(int k=0;k<64;k++) a+=bf2f(h[d*64+k])*bf2f(we[k*64+lane]);
    for(int k=0;k<64;k++) a+=bf2f(h[s*64+k])*bf2f(we[(64+k)*64+lane]);
    a+=d2*bf2f(we[128*64+lane]);
    for(int k=0;k<4;k++) a+=bf2f(ea[e*4+k])*bf2f(we[(129+k)*64+lane]);
    float m=rbf(a/(1.f+__expf(-a)));
    float c=m*bf2f(wx[lane]);
    for(int o=32;o>0;o>>=1) c+=__shfl_xor(c,o);
    atomicAdd(&macc[d*64+lane], m);
    if(lane<3) atomicAdd(&pacc[d*3+lane], df[lane]*c);
  }
}
__global__ void k_node(const uint16_t* h,const uint16_t* pos,const uint16_t* wh,const float* macc,const float* pacc,uint16_t* out,int N){
  int lane=threadIdx.x&63; int n=(blockIdx.x*blockDim.x+threadIdx.x)>>6;
  if(n>=N) return;
  float a=0;
  for(int k=0;k<64;k++) a+=bf2f(h[n*64+k])*bf2f(wh[k*64+lane]);
  for(int k=0;k<64;k++) a+=rbf(macc[n*64+k])*bf2f(wh[(64+k)*64+lane]);
  float s=a/(1.f+__expf(-a));
  out[n*64+lane]=f2bf(bf2f(h[n*64+lane])+s);
  if(lane<3) out[N*64+n*3+lane]=f2bf(bf2f(pos[n*3+lane])+pacc[n*3+lane]);
}

extern "C" void kernel_launch(void* const* d_in, const int* in_sizes, int n_in,
                              void* d_out, int out_size, void* d_ws, size_t ws_size,
                              hipStream_t stream) {
  const uint16_t* h=(const uint16_t*)d_in[0]; const uint16_t* pos=(const uint16_t*)d_in[1]; const uint16_t* ea=(const uint16_t*)d_in[2];
  const int* src=(const int*)d_in[3]; const int* dst=(const int*)d_in[4];
  const uint16_t* we=(const uint16_t*)d_in[5]; const uint16_t* wx=(const uint16_t*)d_in[6]; const uint16_t* wh=(const uint16_t*)d_in[7];
  int N=in_sizes[0]/64; int E=in_sizes[3];
  float* macc=(float*)d_ws; float* pacc=macc+N*64;
  int tot=N*67;
  k_zero<<<(tot+255)/256,256,0,stream>>>(macc,tot);
  k_edge<<<2048,256,0,stream>>>(h,pos,ea,src,dst,we,wx,macc,pacc,E);
  k_node<<<(N*64+255)/256,256,0,stream>>>(h,pos,wh,macc,pacc,(uint16_t*)d_out,N);
}
```

```cpp
#include <hip/hip_runtime.h>
#include <stdint.h>

typedef unsigned int u32;
typedef unsigned short u16;
typedef __attribute__((ext_vector_type(2))) u32 u32x2;
typedef __attribute__((ext_vector_type(3))) u32 u32x3;
typedef __attribute__((ext_vector_type(4))) u32 u32x4;
typedef __attribute__((ext_vector_type(2))) float f32x2;
typedef __attribute__((ext_vector_type(4))) float f32x4;
typedef __attribute__((ext_vector_type(16))) float f32x16;
typedef __attribute__((ext_vector_type(8))) short bf16x8;
typedef __attribute__((ext_vector_type(4))) short s16x4;
#define LAS __attribute__((address_space(3)))
#define DI __device__ __forceinline__

constexpr int NN = 10000, EE = 640000, NB = 256, CH = 2500;
constexpr float NLOG2E = -1.4426950408889634f, NLN2 = -0.6931471805599453f;

constexpr size_t WS_REC = 0;
constexpr size_t WS_TAB = 7680000;
constexpr size_t WS_BT = WS_TAB + 262144;
constexpr size_t WS_WIMG = WS_BT + 1280000;
constexpr size_t WS_WHF = WS_WIMG + 256 * 7168;

DI u32 pk_bf16(float lo, float hi) { u32 r; asm("v_cvt_pk_bf16_f32 %0, %1, %2" : "=v"(r) : "v"(lo), "v"(hi)); return r; }
DI float bf_lo(u32 v) { return __uint_as_float(v << 16); }
DI float bf_hi(u32 v) { return __uint_as_float(v & 0xffff0000u); }
DI int first_node(int b) { return (b * 625 + 15) >> 4; }
DI u32 bucket_of(u32 d) { return (d * 429497u) >> 24; }
#define MFMA(a, b, c) __builtin_amdgcn_mfma_f32_32x32x16_bf16((a), (b), (c), 0, 0, 0)

DI u32 wave_scan_incl(u32 v) {
  v += __builtin_amdgcn_update_dpp(0u, v, 0x111, 0xf, 0xf, false);
  v += __builtin_amdgcn_update_dpp(0u, v, 0x112, 0xf, 0xf, false);
  v += __builtin_amdgcn_update_dpp(0u, v, 0x114, 0xf, 0xf, false);
  v += __builtin_amdgcn_update_dpp(0u, v, 0x118, 0xf, 0xf, false);
  v += __builtin_amdgcn_update_dpp(0u, v, 0x142, 0xa, 0xf, false);
  v += __builtin_amdgcn_update_dpp(0u, v, 0x143, 0xc, 0xf, false);
  return v;
}

__global__ void __launch_bounds__(896) k_part(const u16* __restrict__ h, const u32* __restrict__ ea, const int* __restrict__ src,
                                              const int* __restrict__ dst, const u16* __restrict__ we, const u16* __restrict__ wh,
                                              char* __restrict__ ws) {
  __shared__ __attribute__((aligned(16))) u32 lds[256 + 256 + 7500 + 4];
  u32* hist = lds;
  u32* base = lds + 256;
  u32* stage = lds + 512;
  const int tid = threadIdx.x, lane = tid & 63, t = blockIdx.x;
  if (tid >= 640) {
    const int p = (tid - 640) >> 6, mat = p >> 1, nt = p & 1, r = lane & 31, hh = lane >> 5;
    const int nb0 = first_node(t), nn = first_node(t + 1) - nb0;
    bf16x8 wf[4];
#pragma unroll
    for (int s = 0; s < 4; ++s)
#pragma unroll
      for (int j = 0; j < 8; ++j) wf[s][j] = (short)we[(mat * 64 + 16 * s + 8 * hh + j) * 64 + 32 * nt + r];
    char* wimg = ws + WS_WIMG + (size_t)t * 7168;
#pragma unroll
    for (int mt = 0; mt < 2; ++mt) {
      const int nl = mt * 32 + r, nc = nl < nn ? nl : nn - 1;
      f32x16 acc;
#pragma unroll
      for (int i = 0; i < 16; ++i) acc[i] = 0.f;
#pragma unroll
      for (int s = 0; s < 4; ++s) {
        const bf16x8 hf = *(const bf16x8*)(h + (size_t)(nb0 + nc) * 64 + 16 * s + 8 * hh);
        acc = MFMA(wf[s], hf, acc);
      }
      char* row = mat ? ws + WS_BT + (size_t)(nb0 + nl) * 128 : wimg + (8 + nl) * 128;
      const bool real = nl < nn;
      if (real || (mat == 0 && nl < 48)) {
#pragma unroll
        for (int g = 0; g < 4; ++g) {
          u32x2 o;
          o.x = real ? pk_bf16(acc[4 * g] * NLOG2E, acc[4 * g + 1] * NLOG2E) : 0u;
          o.y = real ? pk_bf16(acc[4 * g + 2] * NLOG2E, acc[4 * g + 3] * NLOG2E) : 0u;
          *(u32x2*)(row + (32 * nt + 8 * g + 4 * hh) * 2) = o;
        }
      }
    }
    {
      const int i = tid - 640, f = i >> 5, c = (i & 31) * 2;
      u32 o = 0;
      if (f < 5) {
        const u32 w = *(const u32*)(we + (f < 4 ? 129 + f : 128) * 64 + c);
        o = pk_bf16(bf_lo(w) * NLOG2E, bf_hi(w) * NLOG2E);
      }
      *(u32*)(wimg + f * 128 + c * 2) = o;
    }
    if (t < 16 && p == 0) {
      const int wnt = t >> 3, s = t & 7;
      bf16x8 f;
#pragma unroll
      for (int j = 0; j < 8; ++j) f[j] = (short)wh[(16 * s + 8 * hh + j) * 64 + 32 * wnt + r];
      *(bf16x8*)(ws + WS_WHF + t * 1024 + lane * 16) = f;
    }
    return;
  }
  const bool act = tid < 625;
  const int e0 = t * CH + 4 * tid;
  u32x4 d4 = {0, 0, 0, 0}, s4 = {0, 0, 0, 0}, a0 = {0, 0, 0, 0}, a1 = {0, 0, 0, 0};
  if (act) {
    d4 = *(const u32x4*)(dst + e0);
    s4 = *(const u32x4*)(src + e0);
    a0 = *(const u32x4*)(ea + 2 * e0);
    a1 = *(const u32x4*)(ea + 2 * e0 + 4);
  }
  if (tid < 256) hist[tid] = 0;
  __syncthreads();
  u32 bk[4], rk[4];
  if (act) {
#pragma unroll
    for (int i = 0; i < 4; ++i) {
      bk[i] = bucket_of(d4[i]);
      rk[i] = atomicAdd(&hist[bk[i]], 1u);
    }
  }
  __syncthreads();
  {
    const u32x4 c = *(const u32x4*)(hist + 4 * lane);
    const u32 tot = c.x + c.y + c.z + c.w;
    const u32 ex = wave_scan_incl(tot) - tot;
    u32x4 b;
    b.x = ex; b.y = ex + c.x; b.z = b.y + c.y; b.w = b.z + c.z;
    *(u32x4*)(base + 4 * lane) = b;
    if (tid < 64) {
      u32* tab = (u32*)(ws + WS_TAB) + ((t & 7) * 32 + (t >> 3));
#pragma unroll
      for (int i = 0; i < 4; ++i) tab[(4 * lane + i) * 256] = b[i] | (c[i] << 16);
    }
  }
  if (act) {
#pragma unroll
    for (int i = 0; i < 4; ++i) {
      const u32 p = (base[bk[i]] + rk[i]) * 3;
      stage[p] = s4[i] | (d4[i] << 14);
      stage[p + 1] = i < 2 ? a0[2 * i] : a1[2 * i - 4];
      stage[p + 2] = i < 2 ? a0[2 * i + 1] : a1[2 * i - 3];
    }
  }
  __syncthreads();
  u32x4* out = (u32x4*)(ws + WS_REC + (size_t)t * (CH * 12));
#pragma unroll
  for (int i = 0; i < 3; ++i) {
    const int q = tid + 640 * i;
    if (q < 1875) out[q] = *(const u32x4*)(stage + 4 * q);
  }
}

constexpr int NW = 8;
constexpr int CAPT = 88;
constexpr int TB = 608;
constexpr int L_STG = 0;
constexpr int L_S = 65536;
constexpr int L_TILE = L_S + NW * 2048;
constexpr int L_MACC = L_TILE + CAPT * TB;
constexpr int L_ZACC = L_MACC + 48 * 272;
constexpr int L_WIMG = L_ZACC + 768;
constexpr int L_LUT = L_WIMG + 7168;
constexpr int L_CNT = L_LUT + 1536;
constexpr int L_PD = L_CNT + 256;
constexpr int L_END = L_PD + 640;
constexpr u32 DL_PAD = 55;

DI void dma16(const void* sbase, u32 voff, u32 ldsaddr) {
  asm volatile("s_mov_b32 m0, %2\n\ts_nop 0\n\tglobal_load_lds_dwordx4 %0, %1" ::"v"(voff), "s"(sbase), "s"(ldsaddr) : "memory");
}

__global__ void __launch_bounds__(512, 2) k_main(const u16* __restrict__ h, const u16* __restrict__ pos, const u16* __restrict__ wx,
                                                 const char* __restrict__ ws, u16* __restrict__ out) {
  extern __shared__ __attribute__((aligned(16))) char lds[];
  const u32 lb = (u32)(size_t)(LAS char*)lds;
  const int tid = threadIdx.x, lane = tid & 63, w = __builtin_amdgcn_readfirstlane(tid >> 6);
  const int b = (blockIdx.x & 7) * 32 + (blockIdx.x >> 3);
  const int nb0 = first_node(b), nn = first_node(b + 1) - nb0;
  const int r = lane & 31, hh = lane >> 5, q = lane >> 4, i16 = lane & 15;

  const u32* tab = (const u32*)(ws + WS_TAB) + b * 256;
  u32 sg[8];
  {
    const u32x4 t0 = *(const u32x4*)(tab + 32 * w + 8 * q), t1 = *(const u32x4*)(tab + 32 * w + 8 * q + 4);
#pragma unroll
    for (int k = 0; k < 4; ++k) { sg[k] = t0[k]; sg[4 + k] = t1[k]; }
  }
  const u32x4 tall = *(const u32x4*)(tab + 4 * lane);
  if (w < 7) dma16(ws + WS_WIMG + (size_t)b * 7168, w * 1024 + lane * 16, lb + L_WIMG + w * 1024);
  for (int i = tid; i < (NW * 2048) / 16; i += 512) *(u32x4*)(lds + L_S + i * 16) = u32x4{0, 0, 0, 0};
  for (int i = tid; i < (48 * 272 + 768) / 16; i += 512) *(u32x4*)(lds + L_MACC + i * 16) = u32x4{0, 0, 0, 0};
  if (tid < 384) {
    const int j = (tid >> 2) - 40, d = tid & 3;
    *(u32*)(lds + L_LUT + tid * 4) = (j >> 1) == d && j >= 0 ? (0x3f80u << (16 * (j & 1))) : 0u;
  }
  if (tid < nn) {
    const u16* p = pos + (nb0 + tid) * 3;
    *(f32x4*)(lds + L_PD + tid * 16) = f32x4{bf_lo(p[0]), bf_lo(p[1]), bf_lo(p[2]), 0.f};
  }
  u32 maxc, total;
  {
    const u32 c0 = tall.x >> 16, c1 = tall.y >> 16, c2 = tall.z >> 16, c3 = tall.w >> 16;
    total = __builtin_amdgcn_readlane(wave_scan_incl(c0 + c1 + c2 + c3), 63);
    u32 m = max(max(c0, c1), max(c2, c3));
#pragma unroll
    for (int o = 32; o > 0; o >>= 1) m = max(m, (u32)__shfl_xor((int)m, o));
    maxc = __builtin_amdgcn_readfirstlane(m);
  }
  const u32 W = (maxc <= 32 && total <= CAPT * 32) ? 32u : 8u;
  const float wx0 = bf_lo(wx[r]), wx1 = bf_lo(wx[32 + r]);

  u32 lo = 0;
  do {
    for (int i = w; i < 59; i += NW)
      if (i * 1024 + lane * 16 < 60000) dma16(pos, i * 1024 + lane * 16, lb + L_STG + i * 1024);
    if (tid < 64) *(u32*)(lds + L_CNT + tid * 4) = 0;
    u32x3 rec[16];
    u32 rank[16];
    bool val[16];
#pragma unroll
    for (int x = 0; x < 2; ++x)
#pragma unroll
      for (int k = 0; k < 8; ++k) {
        const u32 c = sg[k] >> 16, st = sg[k] & 0xffffu, j = lo + i16 + 16 * x;
        const bool v = j < c && (u32)(i16 + 16 * x) < W;
        val[8 * x + k] = v;
        rec[8 * x + k] = u32x3{0, 0, 0};
        if (v) rec[8 * x + k] = *(const u32x3*)(ws + WS_REC + (size_t)((((8 * q + k) << 3) | w) * CH + st + j) * 12);
      }
    __syncthreads();
#pragma unroll
    for (int k = 0; k < 16; ++k) {
      rank[k] = 0;
      if (val[k]) rank[k] = atomicAdd((u32*)(lds + L_CNT) + ((rec[k].x >> 14) - nb0), 1u);
    }
    asm volatile("s_waitcnt vmcnt(0)" ::: "memory");
    __syncthreads();
    const u32 cnt = *(const u32*)(lds + L_CNT + lane * 4);
    const u32 incl = wave_scan_incl(cnt), excl = incl - cnt;
    const u32 T = __builtin_amdgcn_readlane(incl, 63);
    const u32 NT = (T + 31) >> 5;
#pragma unroll
    for (int k = 0; k < 16; ++k) {
      const u32 dl = (rec[k].x >> 14) - nb0, s = rec[k].x & 0x3fffu;
      const u32 bs = __builtin_amdgcn_ds_bpermute(dl * 4, excl);
      if (val[k]) {
        const u32 p = bs + rank[k], sl = p & 31;
        char* tb = lds + L_TILE + (p >> 5) * TB;
        const u32 a = s * 6;
        const u32 d0 = *(const u32*)(lds + L_STG + (a & ~3u)), d1 = *(const u32*)(lds + L_STG + (a & ~3u) + 4);
        const u32 xy = __builtin_amdgcn_alignbyte(d1, d0, a & 2), z = (a & 2) ? (d1 & 0xffff0000u) : (d1 << 16);
        const f32x4 pd = *(const f32x4*)(lds + L_PD + dl * 16);
        const float dx = pd.x - bf_lo(xy), dy = pd.y - bf_hi(xy), dz = pd.z - __uint_as_float(z);
        const float d2 = dx * dx + dy * dy + dz * dz;
        *(u32x4*)(tb + sl * 16) = u32x4{rec[k].y, rec[k].z, pk_bf16(d2, dx), pk_bf16(dy, dz)};
        *(u16*)(tb + 512 + (4 * (sl & 7) + (sl >> 3)) * 2) = (u16)s;
        *(unsigned char*)(tb + 576 + sl) = (unsigned char)dl;
      }
    }
    if (w == 0 && T + lane < NT * 32) {
      const u32 p = T + lane, sl = p & 31;
      char* tb = lds + L_TILE + (p >> 5) * TB;
      *(u32x4*)(tb + sl * 16) = u32x4{0, 0, 0, 0};
      *(u16*)(tb + 512 + (4 * (sl & 7) + (sl >> 3)) * 2) = 0;
      *(unsigned char*)(tb + 576 + sl) = (unsigned char)DL_PAD;
    }
    __syncthreads();

    const u32 tq = NT / NW, tr = NT % NW;
    const u32 t0 = w * tq + min((u32)w, tr), t1 = t0 + tq + (w < tr ? 1 : 0);
    u32 cp = t0 * 32;
    const u32 ep = min(t1 * 32, T);
    const u32 stg = lb + L_STG + w * 8192;
    char* simg = lds + L_S + w * 2048;
    const char* bt = ws + WS_BT;
    while (cp < ep) {
      const u32 nb = __builtin_amdgcn_readfirstlane(*(const unsigned char*)(lds + L_TILE + (cp >> 5) * TB + 576 + (cp & 31)));
      const u32 rp = min(ep, (u32)__builtin_amdgcn_readlane(excl, nb + 8));
      const u32 ta = cp >> 5, tz = (rp + 31) >> 5;
      cp = rp;
      bf16x8 bw[2];
      {
        const int qq = i16 >> 2, pp = i16 & 3, blk = q & 1;
#pragma unroll
        for (int nt = 0; nt < 2; ++nt) {
          const int row = hh ? 8 + nb + qq : qq;
          const LAS char* ap = (const LAS char*)lds + L_WIMG + row * 128 + (32 * nt + 16 * blk + 4 * pp) * 2;
          const s16x4 u0 = __builtin_amdgcn_ds_read_tr16_b64_v4i16((LAS s16x4*)ap);
          const s16x4 u1 = __builtin_amdgcn_ds_read_tr16_b64_v4i16((LAS s16x4*)(ap + 512));
          bw[nt] = __builtin_shufflevector(u0, u1, 0, 1, 2, 3, 4, 5, 6, 7);
        }
      }
      f32x16 y0, y1;
#pragma unroll
      for (int i = 0; i < 16; ++i) { y0[i] = 0.f; y1[i] = 0.f; }
      auto gather = [&](u32 t) {
        const u32x2 wd = *(const u32x2*)(lds + L_TILE + t * TB + 512 + (lane >> 3) * 8);
        const u32 buf = stg + (t & 1) * 4096, co = (lane & 7) * 16;
        dma16(bt, (wd.x & 0xffffu) * 128 + co, buf);
        dma16(bt, (wd.x >> 16) * 128 + co, buf + 1024);
        dma16(bt, (wd.y & 0xffffu) * 128 + co, buf + 2048);
        dma16(bt, (wd.y >> 16) * 128 + co, buf + 3072);
      };
      gather(ta);
      for (u32 t = ta; t < tz; ++t) {
        if (t + 1 < tz) {
          gather(t + 1);
          asm volatile("s_waitcnt vmcnt(4)" ::: "memory");
        } else {
          asm volatile("s_waitcnt vmcnt(0)" ::: "memory");
        }
        const char* tb = lds + L_TILE + t * TB;
        const u32 nl = *(const unsigned char*)(tb + 576 + r) - nb;
        const u32x4 af = *(const u32x4*)(hh ? lds + L_LUT + (40 + (int)nl) * 16 : tb + r * 16);
        char* sp = simg + r * 64 + ((nl ^ ((r >> 1) & 7)) & 7) * 8;
        const bool sw = hh == 0 && nl < 8;
        if (sw) *(u32x2*)sp = u32x2{(af.z & 0xffff0000u) | 0x3f80u, af.w};
        const char* sb = lds + L_STG + w * 8192 + (t & 1) * 4096 + hh * 512 + r * 2;
        f32x16 x0, x1;
#pragma unroll
        for (int i = 0; i < 16; ++i) {
          const int e = (i & 3) + 8 * (i >> 2);
          x0[i] = bf_lo(*(const u16*)(sb + e * 128));
          x1[i] = bf_lo(*(const u16*)(sb + e * 128 + 64));
        }
        const bf16x8 a = __builtin_bit_cast(bf16x8, af);
        x0 = MFMA(a, bw[0], x0);
        x1 = MFMA(a, bw[1], x1);
        bf16x8 sf[2];
        {
          const int qq = i16 >> 2, pp = i16 & 3, blk = q & 1;
#pragma unroll
          for (int s = 0; s < 2; ++s) {
            const int e0 = 16 * s + 4 * hh + qq, e1 = e0 + 8;
            const LAS char* sp0 = (const LAS char*)simg + e0 * 64 + (((4 * blk + pp) ^ ((e0 >> 1) & 7)) * 8);
            const LAS char* sp1 = (const LAS char*)simg + e1 * 64 + (((4 * blk + pp) ^ ((e1 >> 1) & 7)) * 8);
            const s16x4 u0 = __builtin_amdgcn_ds_read_tr16_b64_v4i16((LAS s16x4*)sp0);
            const s16x4 u1 = __builtin_amdgcn_ds_read_tr16_b64_v4i16((LAS s16x4*)sp1);
            sf[s] = __builtin_shufflevector(u0, u1, 0, 1, 2, 3, 4, 5, 6, 7);
          }
        }
        if (sw) *(u32x2*)sp = u32x2{0, 0};
        u32x4 p0[2], p1[2];
#pragma unroll
        for (int i = 0; i < 16; i += 2) {
          const float m0 = x0[i] * __builtin_amdgcn_rcpf(1.f + __builtin_amdgcn_exp2f(x0[i]));
          const float m1 = x0[i + 1] * __builtin_amdgcn_rcpf(1.f + __builtin_amdgcn_exp2f(x0[i + 1]));
          const float m2 = x1[i] * __builtin_amdgcn_rcpf(1.f + __builtin_amdgcn_exp2f(x1[i]));
          const float m3 = x1[i + 1] * __builtin_amdgcn_rcpf(1.f + __builtin_amdgcn_exp2f(x1[i + 1]));
          p0[i >> 3][(i >> 1) & 3] = pk_bf16(m0, m1);
          p1[i >> 3][(i >> 1) & 3] = pk_bf16(m2, m3);
        }
        asm volatile("s_nop 1" : "+v"(p0[0]), "+v"(p0[1]), "+v"(p1[0]), "+v"(p1[1]));
#pragma unroll
        for (int s = 0; s < 2; ++s) {
          y0 = MFMA(sf[s], __builtin_bit_cast(bf16x8, p0[s]), y0);
          y1 = MFMA(sf[s], __builtin_bit_cast(bf16x8, p1[s]), y1);
        }
      }
#pragma unroll
      for (int g = 0; g < 4; ++g) {
        float* mrow = (float*)(lds + L_MACC + (nb + 2 * g + hh) * 272);
        atomicAdd(mrow + r, y0[4 * g]);
        atomicAdd(mrow + 32 + r, y1[4 * g]);
#pragma unroll
        for (int c = 1; c < 4; ++c) {
          float z = y0[4 * g + c] * wx0 + y1[4 * g + c] * wx1;
#pragma unroll
          for (int o = 16; o > 0; o >>= 1) z += __shfl_xor(z, o);
          if (r == 0) atomicAdd((float*)(lds + L_ZACC + (nb + 2 * g + hh) * 16 + c * 4), z);
        }
      }
    }
    lo += W;
    __syncthreads();
  } while (lo < maxc);

  if (w < 4) {
    const int mt = w >> 1, nt = w & 1, nl = mt * 32 + r, nc = nl < nn ? nl : nn - 1;
    const u16* hrow = h + (size_t)(nb0 + nc) * 64;
    f32x16 acc;
#pragma unroll
    for (int i = 0; i < 16; ++i) acc[i] = 0.f;
#pragma unroll
    for (int s = 0; s < 8; ++s) {
      const bf16x8 wf = *(const bf16x8*)(ws + WS_WHF + (nt * 8 + s) * 1024 + lane * 16);
      bf16x8 bf;
      if (s < 4) {
        bf = *(const bf16x8*)(hrow + 16 * s + 8 * hh);
      } else {
        const f32x4 m0 = *(const f32x4*)(lds + L_MACC + nc * 272 + (16 * (s - 4) + 8 * hh) * 4);
        const f32x4 m1 = *(const f32x4*)(lds + L_MACC + nc * 272 + (16 * (s - 4) + 8 * hh) * 4 + 16);
        u32x4 u = {pk_bf16(m0.x * NLN2, m0.y * NLN2), pk_bf16(m0.z * NLN2, m0.w * NLN2), pk_bf16(m1.x * NLN2, m1.y * NLN2),
                   pk_bf16(m1.z * NLN2, m1.w * NLN2)};
        asm volatile("s_nop 1" : "+v"(u));
        bf = __builtin_bit_cast(bf16x8, u);
      }
      acc = MFMA(wf, bf, acc);
    }
    if (nl < nn) {
#pragma unroll
      for (int g = 0; g < 4; ++g) {
        const int c = 32 * nt + 8 * g + 4 * hh;
        const u32x2 hv = *(const u32x2*)(hrow + c);
        float o[4];
#pragma unroll
        for (int j = 0; j < 4; ++j) {
          const float a = acc[4 * g + j];
          o[j] = a * __builtin_amdgcn_rcpf(1.f + __builtin_amdgcn_exp2f(a * NLOG2E));
        }
        *(u32x2*)(out + (size_t)(nb0 + nl) * 64 + c) =
            u32x2{pk_bf16(bf_lo(hv.x) + o[0], bf_hi(hv.x) + o[1]), pk_bf16(bf_lo(hv.y) + o[2], bf_hi(hv.y) + o[3])};
      }
    }
  } else {
    const int i = tid - 256;
    if (i < nn * 3) {
      const int n = i / 3, k = i - 3 * n;
      const float pv = *(const float*)(lds + L_PD + n * 16 + k * 4);
      const float z = *(const float*)(lds + L_ZACC + n * 16 + (k + 1) * 4);
      out[(size_t)NN * 64 + nb0 * 3 + i] = (u16)pk_bf16(pv + NLN2 * z, 0.f);
    }
  }
}

extern "C" void kernel_launch(void* const* d_in, const int* in_sizes, int n_in, void* d_out, int out_size, void* d_ws,
                              size_t ws_size, hipStream_t stream) {
  const u16* h = (const u16*)d_in[0];
  const u16* pos = (const u16*)d_in[1];
  const u32* ea = (const u32*)d_in[2];
  const int* src = (const int*)d_in[3];
  const int* dst = (const int*)d_in[4];
  const u16* we = (const u16*)d_in[5];
  const u16* wx = (const u16*)d_in[6];
  const u16* wh = (const u16*)d_in[7];
  (void)hipFuncSetAttribute((const void*)k_main, hipFuncAttributeMaxDynamicSharedMemorySize, L_END);
  k_part<<<NB, 896, 0, stream>>>(h, ea, src, dst, we, wh, (char*)d_ws);
  k_main<<<NB, 512, L_END, stream>>>(h, pos, wx, (const char*)d_ws, (u16*)d_out);
}
```

```cpp
#include <hip/hip_runtime.h>
#include <stdint.h>

typedef unsigned int u32;
typedef unsigned short u16;
typedef __attribute__((ext_vector_type(2))) u32 u32x2;
typedef __attribute__((ext_vector_type(3))) u32 u32x3;
typedef __attribute__((ext_vector_type(4))) u32 u32x4;
typedef __attribute__((ext_vector_type(2))) float f32x2;
typedef __attribute__((ext_vector_type(4))) float f32x4;
typedef __attribute__((ext_vector_type(16))) float f32x16;
typedef __attribute__((ext_vector_type(8))) short bf16x8;
typedef __attribute__((ext_vector_type(4))) short s16x4;
#define LAS __attribute__((address_space(3)))
#define DI __device__ __forceinline__

constexpr int NN = 10000, EE = 640000, NB = 256, CH = 2500;
constexpr float NLOG2E = -1.4426950408889634f, NLN2 = -0.6931471805599453f;

constexpr size_t WS_REC = 0;
constexpr size_t WS_TAB = 7680000;
constexpr size_t WS_BT = WS_TAB + 262144;
constexpr size_t WS_WIMG = WS_BT + 1280000;
constexpr size_t WS_WHF = WS_WIMG + 256 * 7168;

#define STAMP(K, i)
DI u32 pk_bf16(float lo, float hi) { u32 r; asm("v_cvt_pk_bf16_f32 %0, %1, %2" : "=v"(r) : "v"(lo), "v"(hi)); return r; }
DI float bf_lo(u32 v) { return __uint_as_float(v << 16); }
DI float bf_hi(u32 v) { return __uint_as_float(v & 0xffff0000u); }
DI int first_node(int b) { return (b * 625 + 15) >> 4; }
DI u32 bucket_of(u32 d) { return (d * 429497u) >> 24; }
#define MFMA(a, b, c) __builtin_amdgcn_mfma_f32_32x32x16_bf16((a), (b), (c), 0, 0, 0)

DI u32 wave_scan_incl(u32 v) {
  v += __builtin_amdgcn_update_dpp(0u, v, 0x111, 0xf, 0xf, false);
  v += __builtin_amdgcn_update_dpp(0u, v, 0x112, 0xf, 0xf, false);
  v += __builtin_amdgcn_update_dpp(0u, v, 0x114, 0xf, 0xf, false);
  v += __builtin_amdgcn_update_dpp(0u, v, 0x118, 0xf, 0xf, false);
  v += __builtin_amdgcn_update_dpp(0u, v, 0x142, 0xa, 0xf, false);
  v += __builtin_amdgcn_update_dpp(0u, v, 0x143, 0xc, 0xf, false);
  return v;
}

__global__ void __launch_bounds__(896) k_part(const u16* __restrict__ h, const u32* __restrict__ ea, const int* __restrict__ src,
                                              const int* __restrict__ dst, const u16* __restrict__ we, const u16* __restrict__ wh,
                                              char* __restrict__ ws) {
  __shared__ __attribute__((aligned(16))) u32 lds[256 + 256 + 7500 + 4];
  u32* hist = lds;
  u32* base = lds + 256;
  u32* stage = lds + 512;
  const int tid = threadIdx.x, lane = tid & 63, t = blockIdx.x;
  STAMP(0, 0);
  if (tid >= 640) {
    const int p = (tid - 640) >> 6, mat = p >> 1, nt = p & 1, r = lane & 31, hh = lane >> 5;
    const int nb0 = first_node(t), nn = first_node(t + 1) - nb0;
    bf16x8 wf[4];
#pragma unroll
    for (int s = 0; s < 4; ++s)
#pragma unroll
      for (int j = 0; j < 8; ++j) wf[s][j] = (short)we[(mat * 64 + 16 * s + 8 * hh + j) * 64 + 32 * nt + r];
    char* wimg = ws + WS_WIMG + (size_t)t * 7168;
#pragma unroll
    for (int mt = 0; mt < 2; ++mt) {
      const int nl = mt * 32 + r, nc = nl < nn ? nl : nn - 1;
      f32x16 acc;
#pragma unroll
      for (int i = 0; i < 16; ++i) acc[i] = 0.f;
#pragma unroll
      for (int s = 0; s < 4; ++s) {
        const bf16x8 hf = *(const bf16x8*)(h + (size_t)(nb0 + nc) * 64 + 16 * s + 8 * hh);
        acc = MFMA(wf[s], hf, acc);
      }
      char* row = mat ? ws + WS_BT + (size_t)(nb0 + nl) * 128 : wimg + (8 + nl) * 128;
      const bool real = nl < nn;
      if (real || (mat == 0 && nl < 48)) {
#pragma unroll
        for (int g = 0; g < 4; ++g) {
          u32x2 o;
          o.x = real ? pk_bf16(acc[4 * g] * NLOG2E, acc[4 * g + 1] * NLOG2E) : 0u;
          o.y = real ? pk_bf16(acc[4 * g + 2] * NLOG2E, acc[4 * g + 3] * NLOG2E) : 0u;
          *(u32x2*)(row + (32 * nt + 8 * g + 4 * hh) * 2) = o;
        }
      }
    }
    {
      const int i = tid - 640, f = i >> 5, c = (i & 31) * 2;
      u32 o = 0;
      if (f < 5) {
        const u32 w = *(const u32*)(we + (f < 4 ? 129 + f : 128) * 64 + c);
        o = pk_bf16(bf_lo(w) * NLOG2E, bf_hi(w) * NLOG2E);
      }
      *(u32*)(wimg + f * 128 + c * 2) = o;
    }
    if (t < 16 && p == 0) {
      const int wnt = t >> 3, s = t & 7;
      bf16x8 f;
#pragma unroll
      for (int j = 0; j < 8; ++j) f[j] = (short)wh[(16 * s + 8 * hh + j) * 64 + 32 * wnt + r];
      *(bf16x8*)(ws + WS_WHF + t * 1024 + lane * 16) = f;
    }
    STAMP(0, 7);
    return;
  }
  unsigned long long q0,q1,q2; asm volatile("s_memrealtime %0\n\ts_waitcnt lgkmcnt(0)" : "=s"(q0) :: "memory");
  const bool act = tid < 625;
  const int e0 = t * CH + 4 * tid;
  u32x4 d4 = {0, 0, 0, 0}, s4 = {0, 0, 0, 0}, a0 = {0, 0, 0, 0}, a1 = {0, 0, 0, 0};
  if (act) {
    d4 = *(const u32x4*)(dst + e0);
  }
  asm volatile("" :: "v"(d4));
  asm volatile("s_memrealtime %0\n\ts_waitcnt lgkmcnt(0)" : "=s"(q1) :: "memory");
  if (lane == 0) ((u32*)(ws + (64u<<20)))[t * 16 + (tid >> 6)] = (u32)(q1 - q0);
  if (act) {
    s4 = *(const u32x4*)(src + e0);
    a0 = *(const u32x4*)(ea + 2 * e0);
    a1 = *(const u32x4*)(ea + 2 * e0 + 4);
  }
  if (tid < 256) hist[tid] = 0;
  __syncthreads();
  STAMP(0, 1);
  u32 bk[4], rk[4];
  if (act) {
#pragma unroll
    for (int i = 0; i < 4; ++i) {
      bk[i] = bucket_of(d4[i]);
      rk[i] = atomicAdd(&hist[bk[i]], 1u);
    }
  }
  STAMP(0, 2);
  __syncthreads();
  STAMP(0, 3);
  {
    const u32x4 c = *(const u32x4*)(hist + 4 * lane);
    const u32 tot = c.x + c.y + c.z + c.w;
    const u32 ex = wave_scan_incl(tot) - tot;
    u32x4 b;
    b.x = ex; b.y = ex + c.x; b.z = b.y + c.y; b.w = b.z + c.z;
    *(u32x4*)(base + 4 * lane) = b;
    if (tid < 64) {
      u32* tab = (u32*)(ws + WS_TAB) + ((t & 7) * 32 + (t >> 3));
#pragma unroll
      for (int i = 0; i < 4; ++i) tab[(4 * lane + i) * 256] = b[i] | (c[i] << 16);
    }
  }
  if (act) {
#pragma unroll
    for (int i = 0; i < 4; ++i) {
      const u32 p = (base[bk[i]] + rk[i]) * 3;
      stage[p] = s4[i] | (d4[i] << 14);
      stage[p + 1] = i < 2 ? a0[2 * i] : a1[2 * i - 4];
      stage[p + 2] = i < 2 ? a0[2 * i + 1] : a1[2 * i - 3];
    }
  }
  STAMP(0, 4);
  __syncthreads();
  STAMP(0, 5);
  u32x4* out = (u32x4*)(ws + WS_REC + (size_t)t * (CH * 12));
#pragma unroll
  for (int i = 0; i < 3; ++i) {
    const int q = tid + 640 * i;
    if (q < 1875) out[q] = *(const u32x4*)(stage + 4 * q);
  }
  STAMP(0, 6);
}

constexpr int NW = 8;
constexpr int CAPT = 88;
constexpr int TB = 608;
constexpr int L_STG = 0;
constexpr int L_S = 65536;
constexpr int L_TILE = L_S + NW * 2048;
constexpr int L_MACC = L_TILE + CAPT * TB;
constexpr int L_ZACC = L_MACC + 48 * 272;
constexpr int L_WIMG = L_ZACC + 768;
constexpr int L_LUT = L_WIMG + 7168;
constexpr int L_CNT = L_LUT + 1536;
constexpr int L_PD = L_CNT + 256;
constexpr int L_END = L_PD + 640;
constexpr u32 DL_PAD = 55;

DI void dma16(const void* sbase, u32 voff, u32 ldsaddr) {
  asm volatile("s_mov_b32 m0, %2\n\ts_nop 0\n\tglobal_load_lds_dwordx4 %0, %1" ::"v"(voff), "s"(sbase), "s"(ldsaddr) : "memory");
}

__global__ void __launch_bounds__(512, 2) k_main(const u16* __restrict__ h, const u16* __restrict__ pos, const u16* __restrict__ wx,
                                                 const char* __restrict__ ws, u16* __restrict__ out) {
  extern __shared__ __attribute__((aligned(16))) char lds[];
  const u32 lb = (u32)(size_t)(LAS char*)lds;
  const int tid = threadIdx.x, lane = tid & 63, w = __builtin_amdgcn_readfirstlane(tid >> 6);
  const int b = (blockIdx.x & 7) * 32 + (blockIdx.x >> 3);
  const int nb0 = first_node(b), nn = first_node(b + 1) - nb0;
  const int r = lane & 31, hh = lane >> 5, q = lane >> 4, i16 = lane & 15;

  STAMP(1, 0);
  const u32* tab = (const u32*)(ws + WS_TAB) + b * 256;
  u32 sg[8];
  {
    const u32x4 t0 = *(const u32x4*)(tab + 32 * w + 8 * q), t1 = *(const u32x4*)(tab + 32 * w + 8 * q + 4);
#pragma unroll
    for (int k = 0; k < 4; ++k) { sg[k] = t0[k]; sg[4 + k] = t1[k]; }
  }
  const u32x4 tall = *(const u32x4*)(tab + 4 * lane);
  if (w < 7) dma16(ws + WS_WIMG + (size_t)b * 7168, w * 1024 + lane * 16, lb + L_WIMG + w * 1024);
  for (int i = tid; i < (NW * 2048) / 16; i += 512) *(u32x4*)(lds + L_S + i * 16) = u32x4{0, 0, 0, 0};
  for (int i = tid; i < (48 * 272 + 768) / 16; i += 512) *(u32x4*)(lds + L_MACC + i * 16) = u32x4{0, 0, 0, 0};
  if (tid < 384) {
    const int j = (tid >> 2) - 40, d = tid & 3;
    *(u32*)(lds + L_LUT + tid * 4) = (j >> 1) == d && j >= 0 ? (0x3f80u << (16 * (j & 1))) : 0u;
  }
  if (tid < nn) {
    const u16* p = pos + (nb0 + tid) * 3;
    *(f32x4*)(lds + L_PD + tid * 16) = f32x4{bf_lo(p[0]), bf_lo(p[1]), bf_lo(p[2]), 0.f};
  }
  u32 maxc, total;
  {
    const u32 c0 = tall.x >> 16, c1 = tall.y >> 16, c2 = tall.z >> 16, c3 = tall.w >> 16;
    total = __builtin_amdgcn_readlane(wave_scan_incl(c0 + c1 + c2 + c3), 63);
    u32 m = max(max(c0, c1), max(c2, c3));
#pragma unroll
    for (int o = 32; o > 0; o >>= 1) m = max(m, (u32)__shfl_xor((int)m, o));
    maxc = __builtin_amdgcn_readfirstlane(m);
  }
  const u32 W = (maxc <= 32 && total <= CAPT * 32) ? 32u : 8u;
  const float wx0 = bf_lo(wx[r]), wx1 = bf_lo(wx[32 + r]);

  STAMP(1, 1);
  u32 lo = 0;
  do {
    for (int i = w; i < 59; i += NW)
      if (i * 1024 + lane * 16 < 60000) dma16(pos, i * 1024 + lane * 16, lb + L_STG + i * 1024);
    if (tid < 64) *(u32*)(lds + L_CNT + tid * 4) = 0;
    u32x3 rec[16];
    u32 rank[16];
    bool val[16];
#pragma unroll
    for (int x = 0; x < 2; ++x)
#pragma unroll
      for (int k = 0; k < 8; ++k) {
        const u32 c = sg[k] >> 16, st = sg[k] & 0xffffu, j = lo + i16 + 16 * x;
        const bool v = j < c && (u32)(i16 + 16 * x) < W;
        val[8 * x + k] = v;
        rec[8 * x + k] = u32x3{0, 0, 0};
        if (v) rec[8 * x + k] = *(const u32x3*)(ws + WS_REC + (size_t)((((8 * q + k) << 3) | w) * CH + st + j) * 12);
      }
    STAMP(1, 2);
    __syncthreads();
#pragma unroll
    for (int k = 0; k < 16; ++k) {
      rank[k] = 0;
      if (val[k]) rank[k] = atomicAdd((u32*)(lds + L_CNT) + ((rec[k].x >> 14) - nb0), 1u);
    }
    STAMP(1, 3);
    asm volatile("s_waitcnt vmcnt(0)" ::: "memory");
    __syncthreads();
    STAMP(1, 4);
    const u32 cnt = *(const u32*)(lds + L_CNT + lane * 4);
    const u32 incl = wave_scan_incl(cnt), excl = incl - cnt;
    const u32 T = __builtin_amdgcn_readlane(incl, 63);
    const u32 NT = (T + 31) >> 5;
#pragma unroll
    for (int k = 0; k < 16; ++k) {
      const u32 dl = (rec[k].x >> 14) - nb0, s = rec[k].x & 0x3fffu;
      const u32 bs = __builtin_amdgcn_ds_bpermute(dl * 4, excl);
      if (val[k]) {
        const u32 p = bs + rank[k], sl = p & 31;
        char* tb = lds + L_TILE + (p >> 5) * TB;
        const u32 a = s * 6;
        const u32 d0 = *(const u32*)(lds + L_STG + (a & ~3u)), d1 = *(const u32*)(lds + L_STG + (a & ~3u) + 4);
        const u32 xy = __builtin_amdgcn_alignbyte(d1, d0, a & 2), z = (a & 2) ? (d1 & 0xffff0000u) : (d1 << 16);
        const f32x4 pd = *(const f32x4*)(lds + L_PD + dl * 16);
        const float dx = pd.x - bf_lo(xy), dy = pd.y - bf_hi(xy), dz = pd.z - __uint_as_float(z);
        const float d2 = dx * dx + dy * dy + dz * dz;
        *(u32x4*)(tb + sl * 16) = u32x4{rec[k].y, rec[k].z, pk_bf16(d2, dx), pk_bf16(dy, dz)};
        *(u16*)(tb + 512 + (4 * (sl & 7) + (sl >> 3)) * 2) = (u16)s;
        *(unsigned char*)(tb + 576 + sl) = (unsigned char)dl;
      }
    }
    if (w == 0 && T + lane < NT * 32) {
      const u32 p = T + lane, sl = p & 31;
      char* tb = lds + L_TILE + (p >> 5) * TB;
      *(u32x4*)(tb + sl * 16) = u32x4{0, 0, 0, 0};
      *(u16*)(tb + 512 + (4 * (sl & 7) + (sl >> 3)) * 2) = 0;
      *(unsigned char*)(tb + 576 + sl) = (unsigned char)DL_PAD;
    }
    STAMP(1, 5);
    __syncthreads();
    STAMP(1, 6);

    const u32 tq = NT / NW, tr = NT % NW;
    const u32 t0 = w * tq + min((u32)w, tr), t1 = t0 + tq + (w < tr ? 1 : 0);
    u32 cp = t0 * 32;
    const u32 ep = min(t1 * 32, T);
    const u32 stg = lb + L_STG + w * 8192;
    char* simg = lds + L_S + w * 2048;
    const char* bt = ws + WS_BT;
    while (cp < ep) {
      const u32 nb = __builtin_amdgcn_readfirstlane(*(const unsigned char*)(lds + L_TILE + (cp >> 5) * TB + 576 + (cp & 31)));
      const u32 rp = min(ep, (u32)__builtin_amdgcn_readlane(excl, nb + 8));
      const u32 ta = cp >> 5, tz = (rp + 31) >> 5;
      cp = rp;
      bf16x8 bw[2];
      {
        const int qq = i16 >> 2, pp = i16 & 3, blk = q & 1;
#pragma unroll
        for (int nt = 0; nt < 2; ++nt) {
          const int row = hh ? 8 + nb + qq : qq;
          const LAS char* ap = (const LAS char*)lds + L_WIMG + row * 128 + (32 * nt + 16 * blk + 4 * pp) * 2;
          const s16x4 u0 = __builtin_amdgcn_ds_read_tr16_b64_v4i16((LAS s16x4*)ap);
          const s16x4 u1 = __builtin_amdgcn_ds_read_tr16_b64_v4i16((LAS s16x4*)(ap + 512));
          bw[nt] = __builtin_shufflevector(u0, u1, 0, 1, 2, 3, 4, 5, 6, 7);
        }
      }
      f32x16 y0, y1;
#pragma unroll
      for (int i = 0; i < 16; ++i) { y0[i] = 0.f; y1[i] = 0.f; }
      auto gather = [&](u32 t) {
        const u32x2 wd = *(const u32x2*)(lds + L_TILE + t * TB + 512 + (lane >> 3) * 8);
        const u32 buf = stg + (t & 1) * 4096, co = (lane & 7) * 16;
        dma16(bt, (wd.x & 0xffffu) * 128 + co, buf);
        dma16(bt, (wd.x >> 16) * 128 + co, buf + 1024);
        dma16(bt, (wd.y & 0xffffu) * 128 + co, buf + 2048);
        dma16(bt, (wd.y >> 16) * 128 + co, buf + 3072);
      };
      gather(ta);
      for (u32 t = ta; t < tz; ++t) {
        if (t + 1 < tz) {
          gather(t + 1);
          asm volatile("s_waitcnt vmcnt(4)" ::: "memory");
        } else {
          asm volatile("s_waitcnt vmcnt(0)" ::: "memory");
        }
        const char* tb = lds + L_TILE + t * TB;
        const u32 nl = *(const unsigned char*)(tb + 576 + r) - nb;
        const u32x4 af = *(const u32x4*)(hh ? lds + L_LUT + (40 + (int)nl) * 16 : tb + r * 16);
        char* sp = simg + r * 64 + ((nl ^ ((r >> 1) & 7)) & 7) * 8;
        const bool sw = hh == 0 && nl < 8;
        if (sw) *(u32x2*)sp = u32x2{(af.z & 0xffff0000u) | 0x3f80u, af.w};
        const char* sb = lds + L_STG + w * 8192 + (t & 1) * 4096 + hh * 512 + r * 2;
        f32x16 x0, x1;
#pragma unroll
        for (int i = 0; i < 16; ++i) {
          const int e = (i & 3) + 8 * (i >> 2);
          x0[i] = bf_lo(*(const u16*)(sb + e * 128));
          x1[i] = bf_lo(*(const u16*)(sb + e * 128 + 64));
        }
        const bf16x8 a = __builtin_bit_cast(bf16x8, af);
        x0 = MFMA(a, bw[0], x0);
        x1 = MFMA(a, bw[1], x1);
        bf16x8 sf[2];
        {
          const int qq = i16 >> 2, pp = i16 & 3, blk = q & 1;
#pragma unroll
          for (int s = 0; s < 2; ++s) {
            const int e0 = 16 * s + 4 * hh + qq, e1 = e0 + 8;
            const LAS char* sp0 = (const LAS char*)simg + e0 * 64 + (((4 * blk + pp) ^ ((e0 >> 1) & 7)) * 8);
            const LAS char* sp1 = (const LAS char*)simg + e1 * 64 + (((4 * blk + pp) ^ ((e1 >> 1) & 7)) * 8);
            const s16x4 u0 = __builtin_amdgcn_ds_read_tr16_b64_v4i16((LAS s16x4*)sp0);
            const s16x4 u1 = __builtin_amdgcn_ds_read_tr16_b64_v4i16((LAS s16x4*)sp1);
            sf[s] = __builtin_shufflevector(u0, u1, 0, 1, 2, 3, 4, 5, 6, 7);
          }
        }
        if (sw) *(u32x2*)sp = u32x2{0, 0};
        u32x4 p0[2], p1[2];
#pragma unroll
        for (int i = 0; i < 16; i += 2) {
          const float m0 = x0[i] * __builtin_amdgcn_rcpf(1.f + __builtin_amdgcn_exp2f(x0[i]));
          const float m1 = x0[i + 1] * __builtin_amdgcn_rcpf(1.f + __builtin_amdgcn_exp2f(x0[i + 1]));
          const float m2 = x1[i] * __builtin_amdgcn_rcpf(1.f + __builtin_amdgcn_exp2f(x1[i]));
          const float m3 = x1[i + 1] * __builtin_amdgcn_rcpf(1.f + __builtin_amdgcn_exp2f(x1[i + 1]));
          p0[i >> 3][(i >> 1) & 3] = pk_bf16(m0, m1);
          p1[i >> 3][(i >> 1) & 3] = pk_bf16(m2, m3);
        }
        asm volatile("s_nop 1" : "+v"(p0[0]), "+v"(p0[1]), "+v"(p1[0]), "+v"(p1[1]));
#pragma unroll
        for (int s = 0; s < 2; ++s) {
          y0 = MFMA(sf[s], __builtin_bit_cast(bf16x8, p0[s]), y0);
          y1 = MFMA(sf[s], __builtin_bit_cast(bf16x8, p1[s]), y1);
        }
      }
      STAMP(1, 7);
#pragma unroll
      for (int g = 0; g < 4; ++g) {
        float* mrow = (float*)(lds + L_MACC + (nb + 2 * g + hh) * 272);
        atomicAdd(mrow + r, y0[4 * g]);
        atomicAdd(mrow + 32 + r, y1[4 * g]);
#pragma unroll
        for (int c = 1; c < 4; ++c) {
          float z = y0[4 * g + c] * wx0 + y1[4 * g + c] * wx1;
#pragma unroll
          for (int o = 16; o > 0; o >>= 1) z += __shfl_xor(z, o);
          if (r == 0) atomicAdd((float*)(lds + L_ZACC + (nb + 2 * g + hh) * 16 + c * 4), z);
        }
      }
    }
    lo += W;
    STAMP(1, 8);
    __syncthreads();
    STAMP(1, 9);
  } while (lo < maxc);

  if (w < 4) {
    const int mt = w >> 1, nt = w & 1, nl = mt * 32 + r, nc = nl < nn ? nl : nn - 1;
    const u16* hrow = h + (size_t)(nb0 + nc) * 64;
    f32x16 acc;
#pragma unroll
    for (int i = 0; i < 16; ++i) acc[i] = 0.f;
#pragma unroll
    for (int s = 0; s < 8; ++s) {
      const bf16x8 wf = *(const bf16x8*)(ws + WS_WHF + (nt * 8 + s) * 1024 + lane * 16);
      bf16x8 bf;
      if (s < 4) {
        bf = *(const bf16x8*)(hrow + 16 * s + 8 * hh);
      } else {
        const f32x4 m0 = *(const f32x4*)(lds + L_MACC + nc * 272 + (16 * (s - 4) + 8 * hh) * 4);
        const f32x4 m1 = *(const f32x4*)(lds + L_MACC + nc * 272 + (16 * (s - 4) + 8 * hh) * 4 + 16);
        u32x4 u = {pk_bf16(m0.x * NLN2, m0.y * NLN2), pk_bf16(m0.z * NLN2, m0.w * NLN2), pk_bf16(m1.x * NLN2, m1.y * NLN2),
                   pk_bf16(m1.z * NLN2, m1.w * NLN2)};
        asm volatile("s_nop 1" : "+v"(u));
        bf = __builtin_bit_cast(bf16x8, u);
      }
      acc = MFMA(wf, bf, acc);
    }
    if (nl < nn) {
#pragma unroll
      for (int g = 0; g < 4; ++g) {
        const int c = 32 * nt + 8 * g + 4 * hh;
        const u32x2 hv = *(const u32x2*)(hrow + c);
        float o[4];
#pragma unroll
        for (int j = 0; j < 4; ++j) {
          const float a = acc[4 * g + j];
          o[j] = a * __builtin_amdgcn_rcpf(1.f + __builtin_amdgcn_exp2f(a * NLOG2E));
        }
        *(u32x2*)(out + (size_t)(nb0 + nl) * 64 + c) =
            u32x2{pk_bf16(bf_lo(hv.x) + o[0], bf_hi(hv.x) + o[1]), pk_bf16(bf_lo(hv.y) + o[2], bf_hi(hv.y) + o[3])};
      }
    }
  } else {
    const int i = tid - 256;
    if (i < nn * 3) {
      const int n = i / 3, k = i - 3 * n;
      const float pv = *(const float*)(lds + L_PD + n * 16 + k * 4);
      const float z = *(const float*)(lds + L_ZACC + n * 16 + (k + 1) * 4);
      out[(size_t)NN * 64 + nb0 * 3 + i] = (u16)pk_bf16(pv + NLN2 * z, 0.f);
    }
  }
  { u32 T = 0; for (int i = 0; i < 10; ++i) T = max(T, ((const u32*)(ws + (64u<<20)))[blockIdx.x * 16 + i]); T = min(T, 2000u); unsigned long long a = __builtin_amdgcn_s_memrealtime(); while (__builtin_amdgcn_s_memrealtime() - a < 50ull * T) __builtin_amdgcn_s_sleep(2); }
}

extern "C" void kernel_launch(void* const* d_in, const int* in_sizes, int n_in, void* d_out, int out_size, void* d_ws,
                              size_t ws_size, hipStream_t stream) {
  const u16* h = (const u16*)d_in[0];
  const u16* pos = (const u16*)d_in[1];
  const u32* ea = (const u32*)d_in[2];
  const int* src = (const int*)d_in[3];
  const int* dst = (const int*)d_in[4];
  const u16* we = (const u16*)d_in[5];
  const u16* wx = (const u16*)d_in[6];
  const u16* wh = (const u16*)d_in[7];
  (void)hipFuncSetAttribute((const void*)k_main, hipFuncAttributeMaxDynamicSharedMemorySize, L_END);
  k_part<<<NB, 896, 0, stream>>>(h, ea, src, dst, we, wh, (char*)d_ws);
  k_main<<<NB, 512, L_END, stream>>>(h, pos, wx, (const char*)d_ws, (u16*)d_out);
}
```

```cpp
#include <hip/hip_runtime.h>
#include <stdint.h>

typedef unsigned int u32;
typedef unsigned short u16;
typedef unsigned long long u64;
typedef __attribute__((ext_vector_type(2))) u32 u32x2;
typedef __attribute__((ext_vector_type(4))) u32 u32x4;
typedef __attribute__((ext_vector_type(8))) u32 u32x8;
typedef __attribute__((ext_vector_type(16))) u32 u32x16;
typedef __attribute__((ext_vector_type(4))) float f32x4;
typedef __attribute__((ext_vector_type(16))) float f32x16;
typedef __attribute__((ext_vector_type(8))) short bf16x8;
typedef __attribute__((ext_vector_type(4))) short s16x4;
#define LAS __attribute__((address_space(3)))
#define DI __device__ __forceinline__

constexpr int NN = 10000, NB = 256, CH = 2500;
constexpr float NLOG2E = -1.4426950408889634f, NLN2 = -0.6931471805599453f;

constexpr u32 WS_GEN = 0;
constexpr u32 WS_FLG = 4096;
constexpr u32 WS_TAB = WS_FLG + 32768;
constexpr u32 WS_REC = WS_TAB + 524288;
constexpr u32 RS = 4096;
constexpr u32 WS_BT = WS_REC + NB * RS * 4;

DI u32 pk_bf16(float lo, float hi) { u32 r; asm("v_cvt_pk_bf16_f32 %0, %1, %2" : "=v"(r) : "v"(lo), "v"(hi)); return r; }
DI float bf_lo(u32 v) { return __uint_as_float(v << 16); }
DI float bf_hi(u32 v) { return __uint_as_float(v & 0xffff0000u); }
DI int first_node(int b) { return (b * 625 + 15) >> 4; }
#define MFMA16(a, b, c) __builtin_amdgcn_mfma_f32_16x16x32_bf16(__builtin_bit_cast(bf16x8, a), __builtin_bit_cast(bf16x8, b), (c), 0, 0, 0)
DI void lds_barrier() { asm volatile("s_waitcnt lgkmcnt(0)\n\ts_barrier" ::: "memory"); }

DI u32 wave_scan_incl(u32 v) {
  v += __builtin_amdgcn_update_dpp(0u, v, 0x111, 0xf, 0xf, false);
  v += __builtin_amdgcn_update_dpp(0u, v, 0x112, 0xf, 0xf, false);
  v += __builtin_amdgcn_update_dpp(0u, v, 0x114, 0xf, 0xf, false);
  v += __builtin_amdgcn_update_dpp(0u, v, 0x118, 0xf, 0xf, false);
  v += __builtin_amdgcn_update_dpp(0u, v, 0x142, 0xa, 0xf, false);
  v += __builtin_amdgcn_update_dpp(0u, v, 0x143, 0xc, 0xf, false);
  return v;
}
DI u32x2 tr_read(u32 addr) {
  return __builtin_bit_cast(u32x2, __builtin_amdgcn_ds_read_tr16_b64_v4i16((LAS s16x4*)(size_t)addr));
}

constexpr int NW = 8;
constexpr int NS = 7;
constexpr int CAP = 3072;
constexpr int L_LUT = 0;
constexpr int L_CNT = L_LUT + 128;
constexpr int L_T = L_CNT + 256;
constexpr int L_WIMG = L_T + 640;
constexpr int L_NW = L_WIMG + 7168;
constexpr int L_F = L_NW + 128;
constexpr int L_MACC = L_F + CAP * 16;
constexpr int L_ZACC = L_MACC + 80 * 272;
constexpr int L_S = L_ZACC + 1280 + 128;
constexpr int L_STG = L_S + NW * 2048;
constexpr int L_HB = L_STG + 60416;
constexpr int L_SINK = L_STG + 65536;
constexpr int L_END = L_SINK + 256;
constexpr int L_W12 = L_F;
constexpr int L_LIST = L_F + 24576;
constexpr int L_RST = L_F + 24576;
constexpr int L_WH = L_S;
constexpr int L_HIST = L_F + 36864;
constexpr int L_BASE = L_HIST + 1024;
static_assert(NS == 7 && CAP / NW == 64 * (NS - 1) && L_W12 % 128 == 0 && L_S % 256 == 0 && L_F % 16 == 0 && L_END <= 163840, "lds map");

DI void dma16(const void* sbase, u32 voff, u32 ldsaddr) {
  asm volatile("s_mov_b32 m0, %2\n\ts_nop 0\n\tglobal_load_lds_dwordx4 %0, %1" ::"v"(voff), "s"(sbase), "s"(ldsaddr) : "memory");
}
DI void dma16x4(const void* sbase, u32 voff, u32 ldsaddr) {
  asm volatile(
      "s_mov_b32 m0, %2\n\ts_nop 0\n\tglobal_load_lds_dwordx4 %0, %1\n\tglobal_load_lds_dwordx4 %0, %1 offset:1024\n\t"
      "global_load_lds_dwordx4 %0, %1 offset:2048\n\tglobal_load_lds_dwordx4 %0, %1 offset:3072" ::"v"(voff), "s"(sbase), "s"(ldsaddr)
      : "memory");
}
DI void dma16x4w(const void* sbase, u32 voff, u32 ldsaddr) {
  asm volatile(
      "s_mov_b32 m0, %3\n\ts_nop 0\n\tglobal_load_lds_dwordx4 %0, %2\n\tglobal_load_lds_dwordx4 %1, %2 offset:1024\n\t"
      "global_load_lds_dwordx4 %0, %2 offset:2048\n\tglobal_load_lds_dwordx4 %1, %2 offset:3072" ::"v"(voff), "v"(voff ^ 64), "s"(sbase),
      "s"(ldsaddr)
      : "memory");
}
DI u32 poll_tags(u32x2& x, u32 ofs, const char* base, u64 tag) {
  u64 save, tmp;
  u32 left = 1u << 22;
  asm volatile(
      "s_mov_b64 %1, exec\n\t"
      "LQ_%=:\n\t"
      "v_lshrrev_b64 %2, 24, %0\n\t"
      "v_cmpx_ne_u64 vcc, %6, %2\n\t"
      "s_cbranch_execz LR_%=\n\t"
      "global_load_dwordx2 %0, %4, %5 sc1\n\t"
      "s_sub_u32 %3, %3, 1\n\t"
      "s_waitcnt vmcnt(0)\n\t"
      "s_cbranch_scc0 LQ_%=\n\t"
      "v_mov_b64 %0, 0\n\t"
      "LR_%=:\n\t"
      "s_mov_b64 exec, %1\n\ts_nop 2"
      : "+v"(x), "=&s"(save), "=&v"(tmp), "+s"(left)
      : "v"(ofs), "s"(base), "s"(tag)
      : "vcc", "scc", "memory");
  return left >> 31;
}
DI void touch(const void* sbase, u32 voff, u32 ldsaddr) {
  asm volatile("s_mov_b32 m0, %2\n\ts_nop 0\n\tglobal_load_lds_dword %0, %1 sc1" ::"v"(voff), "s"(sbase), "s"(ldsaddr) : "memory");
}
#define GLD(ins, dstv, sbase, voff) asm volatile(ins : "=v"(dstv) : "v"(voff), "s"(sbase) : "memory")
DI void st16_dev(void* sbase, u32 voff, u32x4 x) {
  asm volatile("global_store_dwordx4 %0, %1, %2 sc1\n\ts_nop 1" ::"v"(voff), "v"(x), "s"(sbase) : "memory");
}

__global__ void __launch_bounds__(512, 2)
    k_egnn(const u16* __restrict__ h, const u16* __restrict__ pos, const u16* __restrict__ ea, const int* __restrict__ src,
           const int* __restrict__ dst, const u16* __restrict__ we, const u16* __restrict__ wx, const u16* __restrict__ wh, char* ws,
           u16* __restrict__ out) {
  extern __shared__ __attribute__((aligned(16))) char lds[];
  const u32 lb = (u32)(size_t)(LAS char*)lds;
  const int w = __builtin_amdgcn_readfirstlane(threadIdx.x >> 6);
  const int t = blockIdx.x, b = (t & 7) * 32 + (t >> 3);
  const int nb0 = first_node(b), nn = first_node(b + 1) - nb0;
#define LANE_IDS                                                                                \
  int lane = __builtin_amdgcn_mbcnt_hi(~0u, __builtin_amdgcn_mbcnt_lo(~0u, 0u));                \
  asm volatile("" : "+v"(lane));                                                                \
  const int tid = w * 64 + lane, r = lane & 31, hh = lane >> 5, q = lane >> 4, i16 = lane & 15; \
  const int qq = i16 >> 2, pp = i16 & 3, blk = q & 1;                                           \
  (void)tid; (void)r; (void)hh; (void)q; (void)i16; (void)qq; (void)pp; (void)blk

  auto load_pos = [&]() {
    LANE_IDS;
    if (w < 7) {
#pragma unroll
      for (int i = 0; i < 2; ++i) dma16x4(pos, w * 8192 + i * 4096 + lane * 16, lb + L_STG + w * 8192 + i * 4096);
    } else {
#pragma unroll
      for (int i = 56; i < 59; ++i) dma16(pos, min(i * 1024 + lane * 16, 59984), lb + L_STG + i * 1024);
    }
  };
  struct NodeTerm { u32x4 h0, h1; u32x2 a[8]; };
  auto node_term = [&](const NodeTerm& n) {
    u32x2 o[2];
#pragma unroll
    for (int ct = 0; ct < 2; ++ct) {
      const u32x2* a = n.a + 4 * ct;
      const f32x4 acc = MFMA16((u32x4{a[2].x, a[2].y, a[3].x, a[3].y}), n.h1,
                               MFMA16((u32x4{a[0].x, a[0].y, a[1].x, a[1].y}), n.h0, (f32x4{0.f, 0.f, 0.f, 0.f})));
      o[ct] = u32x2{pk_bf16(acc.x * NLOG2E, acc.y * NLOG2E), pk_bf16(acc.z * NLOG2E, acc.w * NLOG2E)};
    }
    asm volatile("s_nop 1" : "+v"(o[0]), "+v"(o[1]));
    const auto x = __builtin_amdgcn_permlane16_swap(o[0].x, o[1].x, false, false);
    const auto y = __builtin_amdgcn_permlane16_swap(o[0].y, o[1].y, false, false);
    return u32x4{x[0], y[0], x[1], y[1]};
  };
  u32 wx0, wx1, taglo, taghi;
  u32x4 wm[4] = {};
  f32x4 hacc[2] = {};
  u32x2 hres[2] = {};
  u32 seg, pre, nw;
  u32 R0 = 0, nmax = 0, nr;
  u32x16 cv;
  u32x8 id;
  u32x2 trv;
  u32 zsel;
  u32 wcap = 64 * NS;
  u32 rec[NS], off[NS];
  auto make_list = [&]() {
    LANE_IDS;
    nr = __builtin_amdgcn_readfirstlane(min(nw - min(nw, R0), wcap));
    char* list = lds + L_LIST + w * (NS * 256);
    const int k0 = max((int)(R0 - pre), 0) + (lane & 1), k1 = min((int)(seg >> 12), (int)(R0 + wcap - pre));
    u32 ent = (u32)(32 * w + (lane >> 1)) << 14 | ((seg & 0xfffu) + k0) << 2;
    int left = k1 - k0;
    u32 la = (u32)(size_t)(LAS u32*)((u32*)list + (pre - R0 + k0));
    u64 save;
    asm volatile(
        "s_mov_b64 %3, exec\n\t"
        "LX_%=:\n\t"
        "v_cmpx_lt_i32 vcc, 0, %0\n\tds_write_b32 %1, %2\n\tv_add_u32 %2, 8, %2\n\t"
        "v_cmpx_lt_i32 vcc, 2, %0\n\tds_write_b32 %1, %2 offset:8\n\tv_add_u32 %2, 8, %2\n\t"
        "v_cmpx_lt_i32 vcc, 4, %0\n\tds_write_b32 %1, %2 offset:16\n\tv_add_u32 %2, 8, %2\n\t"
        "v_cmpx_lt_i32 vcc, 6, %0\n\tds_write_b32 %1, %2 offset:24\n\tv_add_u32 %2, 8, %2\n\t"
        "v_cmpx_lt_i32 vcc, 8, %0\n\t"
        "v_add_u32 %0, -8, %0\n\tv_add_u32 %1, 32, %1\n\t"
        "s_cbranch_execnz LX_%=\n\t"
        "s_mov_b64 exec, %3\n\ts_nop 2"
        : "+v"(left), "+v"(la), "+v"(ent), "=&s"(save)::"vcc", "memory");
#pragma unroll
    for (int i = 0; i < NS; ++i) off[i] = *(const u32*)(list + (lane + 64 * i) * 4);
    asm volatile("" : "+v"(off[0]), "+v"(off[1]), "+v"(off[2]), "+v"(off[3]), "+v"(off[4]), "+v"(off[5]), "+v"(off[6]));
  };
  auto load_recs = [&]() {
    asm volatile(
        "s_cmp_ge_u32 %[nf], 4\n\ts_cbranch_scc0 LG_%=\n\t"
        "global_load_dword %0, %7, %[b] sc1\n\t"
        "global_load_dword %1, %8, %[b] sc1\n\t"
        "global_load_dword %2, %9, %[b] sc1\n\t"
        "global_load_dword %3, %10, %[b] sc1\n\t"
        "s_cmp_gt_u32 %[nf], 4\n\ts_cselect_b64 exec, -1, %[pm]\n\ts_cbranch_execz LE_%=\n\tglobal_load_dword %4, %11, %[b] sc1\n\ts_cbranch_scc0 LE_%=\n\t"
        "s_cmp_gt_u32 %[nf], 5\n\ts_cselect_b64 exec, -1, %[pm]\n\ts_cbranch_execz LE_%=\n\tglobal_load_dword %5, %12, %[b] sc1\n\ts_cbranch_scc0 LE_%=\n\t"
        "s_cmp_gt_u32 %[nf], 6\n\ts_cselect_b64 exec, -1, %[pm]\n\ts_cbranch_execz LE_%=\n\tglobal_load_dword %6, %13, %[b] sc1\n\t"
        "s_branch LE_%=\n\tLG_%=:\n\t"
        "s_cmp_gt_u32 %[nf], 0\n\ts_cselect_b64 exec, -1, %[pm]\n\ts_cbranch_execz LE_%=\n\tglobal_load_dword %0, %7, %[b] sc1\n\ts_cbranch_scc0 LE_%=\n\t"
        "s_cmp_gt_u32 %[nf], 1\n\ts_cselect_b64 exec, -1, %[pm]\n\ts_cbranch_execz LE_%=\n\tglobal_load_dword %1, %8, %[b] sc1\n\ts_cbranch_scc0 LE_%=\n\t"
        "s_cmp_gt_u32 %[nf], 2\n\ts_cselect_b64 exec, -1, %[pm]\n\ts_cbranch_execz LE_%=\n\tglobal_load_dword %2, %9, %[b] sc1\n\ts_cbranch_scc0 LE_%=\n\t"
        "s_cmp_gt_u32 %[nf], 3\n\ts_cselect_b64 exec, -1, %[pm]\n\ts_cbranch_execz LE_%=\n\tglobal_load_dword %3, %10, %[b] sc1\n\t"
        "LE_%=:\n\ts_mov_b64 exec, -1"
        : "=&v"(rec[0]), "=&v"(rec[1]), "=&v"(rec[2]), "=&v"(rec[3]), "=&v"(rec[4]), "=&v"(rec[5]), "=&v"(rec[6])
        : "v"(off[0]), "v"(off[1]), "v"(off[2]), "v"(off[3]), "v"(off[4]), "v"(off[5]), "v"(off[6]), [b] "s"(ws + WS_REC),
          [nf] "s"(nr >> 6), [pm] "s"((1ull << (nr & 63)) - 1)
        : "scc", "memory");
  };
  {
    LANE_IDS;
    u32 d[5], s[5], feat, pdx, pdy, pdz;
    u32x2 gen;
    const u32 wl = lane * 16 ^ (lane & 16) * 2;
    {
      const int row = 8 * min(w, 4) + (lane >> 3);
      dma16(h, (nb0 + min(row, nn - 1)) * 128 + (((lane & 7) ^ ((row >> 1) & 7)) * 16), lb + L_HB + min(w, 4) * 1024);
      dma16(we, 8192 + w * 1024 + (wl ^ (w & 1) * 64), lb + L_W12 + 8192 + w * 1024);
    }
#pragma unroll
    for (int i = 0; i < 5; ++i) GLD("global_load_dword %0, %1, %2", d[i], dst + t * CH, (u32)min(tid + 512 * i, CH - 1) * 4);
    GLD("global_load_dwordx2 %0, %1, %2 sc1", gen, ws, WS_GEN);
#pragma unroll
    for (int i = 0; i < 5; ++i) GLD("global_load_dword %0, %1, %2", s[i], src + t * CH, (u32)min(tid + 512 * i, CH - 1) * 4);
    {
      const int f = min(tid >> 5, 4);
      GLD("global_load_dword %0, %1, %2", feat, we, (u32)((f < 4 ? 129 + f : 128) * 128 + r * 4));
      const u32 po = (nb0 + min(tid, nn - 1)) * 6;
      GLD("global_load_short_d16_hi %0, %1, %2", pdx, pos, po);
      GLD("global_load_short_d16_hi %0, %1, %2 offset:2", pdy, pos, po);
      GLD("global_load_short_d16_hi %0, %1, %2 offset:4", pdz, pos, po);
      GLD("global_load_short_d16_hi %0, %1, %2", wx0, wx, (u32)(2 * r));
      GLD("global_load_short_d16_hi %0, %1, %2 offset:64", wx1, wx, (u32)(2 * r));
    }
    if (tid < 256) *(u32*)(lds + L_HIST + tid * 4) = 0;
#pragma unroll
    for (int i = 0; i < 3; ++i)
      if (i < 2 || tid + 512 * i < (L_S - L_MACC) / 16) *(u32x4*)(lds + L_MACC + (tid + 512 * i) * 16) = u32x4{0, 0, 0, 0};
    if (tid < 96) {
      const int n = (tid >> 1) & 7, j = n - 4 * (tid >> 4);
      *(u32*)(lds + L_LUT + tid * 4) = (tid < 32 && j >= 0 && (j >> 1) == (tid & 1)) ? (0x3f80u << (16 * (j & 1))) : 0u;
    }
    u32 tabofs = (32 * w + (lane >> 1)) * 2048 + b * 8;
    u32 btofs = WS_BT + (nb0 + (w >> 1) * 16 + i16) * 128 + (w & 1) * 64 + (q & 1) * 32 + (q >> 1) * 16;
    u32 ntw = lb + L_W12 + (8 * q + qq) * 128 + (((w & 1) * 64 + 8 * pp) ^ ((qq >> 1) * 32 + (q & 1) * 64)), nth0, nth1;
    {
      const int hr = min((w >> 1) * 16 + i16, 39), sw = (hr >> 1) & 7;
      nth0 = L_HB + hr * 128 + (q ^ sw) * 16;
      nth1 = nth0 ^ 64;
    }
    asm volatile("" : "+v"(tabofs), "+v"(btofs), "+v"(ntw), "+v"(nth0), "+v"(nth1));
    auto node_term_load = [&](NodeTerm& n, int wofs) {
      n.h0 = *(const u32x4*)(lds + nth0);
      n.h1 = *(const u32x4*)(lds + nth1);
#pragma unroll
      for (int i = 0; i < 8; ++i) n.a[i] = tr_read((ntw ^ (i >> 2) * 32) + wofs + (i & 2) * 2048 + (i & 1) * 512);
    };
    asm volatile("s_waitcnt vmcnt(17)" ::: "memory");
    lds_barrier();
    if (w < 6) {
      NodeTerm nt;
      node_term_load(nt, 8192);
      const u32x4 row = node_term(nt);
      if ((w >> 1) * 16 + i16 < nn) st16_dev(ws, btofs, row);
      asm volatile("s_waitcnt vmcnt(11)" ::: "memory");
    } else {
      dma16x4w(we, (w & 1) * 4096 + wl, lb + L_W12 + (w & 1) * 4096);
#pragma unroll
      for (int i = 0; i < 2; ++i) dma16x4w(wh, ((w & 1) * 2 + i) * 4096 + wl, lb + L_WH + ((w & 1) * 2 + i) * 4096);
      touch(pos, (u32)min(((t >> 3) * 16 + (w & 1) * 8 + (lane & 7)) * 128, 59996), lb + L_SINK);
      asm volatile("s_waitcnt vmcnt(23)" ::: "memory");
    }
    asm volatile("" : "+v"(d[0]), "+v"(d[1]), "+v"(d[2]), "+v"(d[3]), "+v"(d[4]), "+v"(gen)::"memory");
    u32 bk[5], rk[5];
#pragma unroll
    for (int i = 0; i < 5; ++i) {
      bk[i] = (u32)__umul24(d[i], 429497u) >> 24;
      rk[i] = 0;
      if (i < 4 || tid < CH - 2048) rk[i] = atomicAdd((u32*)(lds + L_HIST) + bk[i], 1u);
    }
#pragma unroll
    for (int i = 0; i < 5; ++i) d[i] = (d[i] - ((bk[i] * 625 + 15) >> 4)) | (u32)(tid + 512 * i) << 20;
    asm volatile("" : "+v"(d[0]), "+v"(d[1]), "+v"(d[2]), "+v"(d[3]), "+v"(d[4]));
    const u64 g1 = ((u64)(u32)__builtin_amdgcn_readfirstlane(gen.y) << 32 | (u32)__builtin_amdgcn_readfirstlane(gen.x)) + 1;
    taglo = (u32)g1 << 24;
    taghi = (u32)(g1 >> 8);
    lds_barrier();
    const u32x4 c = *(const u32x4*)(lds + L_HIST + 16 * lane);
    u32x4 bs;
    {
      const u32 tot = c.x + c.y + c.z + c.w;
      bs.x = wave_scan_incl(tot) - tot;
      bs.y = bs.x + c.x; bs.z = bs.y + c.y; bs.w = bs.z + c.z;
      *(u32x4*)(lds + L_BASE + 16 * lane) = bs;
    }
    u32 ps[5];
#pragma unroll
    for (int i = 0; i < 5; ++i) ps[i] = *(const u32*)(lds + L_BASE + bk[i] * 4);
    if (w >= 6) {
      const u32 s0 = w & 1 ? bs.z : bs.x, s1 = w & 1 ? bs.w : bs.y, c0 = w & 1 ? c.z : c.x, c1 = w & 1 ? c.w : c.y;
      st16_dev(ws + WS_TAB + t * 2048, lane * 32 + (w & 1) * 16, u32x4{taglo | c0 << 12 | s0, taghi, taglo | c1 << 12 | s1, taghi});
    }
    asm volatile("s_waitcnt vmcnt(1)" ::: "memory");
    asm volatile(""
                 : "+v"(s[0]), "+v"(s[1]), "+v"(s[2]), "+v"(s[3]), "+v"(s[4]), "+v"(feat), "+v"(pdx), "+v"(pdy), "+v"(pdz), "+v"(wx0),
                   "+v"(wx1)::"memory");
#pragma unroll
    for (int i = 0; i < 5; ++i)
      if (i < 4 || tid < CH - 2048) *(u32*)(lds + L_RST + (ps[i] + rk[i]) * 4) = s[i] << 6 | d[i];
    lds_barrier();
    st16_dev(ws + WS_REC + t * (RS * 4), tid * 16, *(const u32x4*)(lds + L_RST + tid * 16));
    if (w >= 6) {
      const int i2 = min(tid + 128, CH / 4 - 1);
      st16_dev(ws + WS_REC + t * (RS * 4), i2 * 16, *(const u32x4*)(lds + L_RST + i2 * 16));
    }
    if (tid < 256) *(u32*)(lds + L_WIMG + tid * 4) = tid < 128 ? pk_bf16(bf_lo(feat) * NLOG2E, bf_hi(feat) * NLOG2E) : tid < 160 ? feat : 0u;
    if (tid < 40) *(u32x4*)(lds + L_T + tid * 16) = u32x4{pdx, pdy, pdz, 0u};
    if (w < 6) {
      NodeTerm na;
      node_term_load(na, 0);
      const u32 ta[2] = {ntw - L_W12 + L_WH, (ntw ^ 32) - L_W12 + L_WH};
      const int hr = min((w >> 1) * 16 + i16, 39), sw = (hr >> 1) & 7;
      const char* hp = lds + L_HB + hr * 128;
#pragma unroll
      for (int ct = 0; ct < 2; ++ct) hres[ct] = *(const u32x2*)(hp + (((w & 1) * 4 + ct * 2 + (q >> 1)) ^ sw) * 16 + (q & 1) * 8);
#pragma unroll
      for (int ct = 0; ct < 2; ++ct) {
        u32x4 a[4];
#pragma unroll
        for (int k = 0; k < 4; ++k) {
          const u32x2 a0 = tr_read(ta[ct] + k * 4096), a1 = tr_read(ta[ct] + k * 4096 + 512);
          a[k] = u32x4{a0.x, a0.y, a1.x, a1.y};
        }
        hacc[ct] = MFMA16(a[1], na.h1, MFMA16(a[0], na.h0, (f32x4{0.f, 0.f, 0.f, 0.f})));
        wm[2 * ct] = a[2];
        wm[2 * ct + 1] = a[3];
      }
      *(u32x4*)(lds + L_WIMG + 1024 + ((w >> 1) * 16 + i16) * 128 + (w & 1) * 64 + (q & 1) * 32 + (q >> 1) * 16) = node_term(na);
    }
    asm volatile("s_waitcnt vmcnt(0)" ::: "memory");
    lds_barrier();
    const u64 tag = (u64)(taghi >> 24) << 32 | (taghi << 8 | taglo >> 24);
    u32x2 g;
    GLD("global_load_dwordx2 %0, %1, %2 sc1", g, ws + WS_TAB, tabofs);
    if (w == 0) {
      const u32x2 f = {taglo, taghi};
      if (lane == 0) asm volatile("global_store_dwordx2 %0, %1, %2 sc1" ::"v"((u32)(t * 128)), "v"(f), "s"(ws + WS_FLG) : "memory");
    }
    load_pos();
    if (w == 0) asm volatile("s_waitcnt vmcnt(9)" ::: "memory");
    else if (w < 7) asm volatile("s_waitcnt vmcnt(8)" ::: "memory");
    else asm volatile("s_waitcnt vmcnt(3)" ::: "memory");
    asm volatile("" : "+v"(g)::"memory");
    u32 bad = poll_tags(g, tabofs, ws + WS_TAB, tag);
    const u32 flgofs = (32 * w + r) * 128;
    u32x2 fl;
    GLD("global_load_dwordx2 %0, %1, %2 sc1", fl, ws + WS_FLG, flgofs);
    seg = g.x & 0xffffffu;
    const u32 cs = seg >> 12, incl = wave_scan_incl((lane & 1) ? 0u : cs);
    pre = incl - cs;
    nw = __builtin_amdgcn_readlane(incl, 63);
    make_list();
    asm volatile("s_waitcnt vmcnt(0)" : "+v"(fl)::"memory");
    bad |= poll_tags(fl, flgofs, ws + WS_FLG, tag);
    load_recs();
    if (w >= 5) touch(ea + t * (CH * 4), (u32)min((tid - 320) * 128, CH * 8 - 4), lb + L_SINK);
#pragma unroll
    for (int i = 0; i < 2; ++i) *(u32x4*)(lds + L_S + w * 2048 + i * 1024 + lane * 16) = u32x4{0, 0, 0, 0};
    {
      const u32 stg = lb + L_STG + w * 4096, simg = lb + L_S + w * 2048;
      cv[0] = cv[1] = 0;
      cv[2] = simg + r * 64 + ((r >> 1) & 7) * 8;
      cv[3] = lb + L_LUT + 64 * hh;
      trv.x = simg + (4 * hh + qq) * 64 + (((4 * blk + pp) ^ (2 * hh + (qq >> 1))) * 8);
      trv.y = trv.x ^ 32;
#pragma unroll
      for (int i = 0; i < 4; ++i) {
        cv[4 + 2 * i] = 0;
        cv[5 + 2 * i] = (((lane & 7) ^ (i + 4 * hh)) * 16) + 3072 - 1024 * i;
        cv[12 + i] = stg + (8 * (r & 3) + (r >> 2)) * 128 + (((2 * i + hh) ^ ((r & 3) + 4 * (r >> 4))) * 16);
        const u32 one = ((r >> 1) & 7) == 4 * hh + i ? 0x3f80u << (16 * (r & 1)) : 0u;
        id[i] = r < 16 ? one : 0u;
        id[4 + i] = r < 16 ? 0u : one;
      }
      const int j = (i16 & 2) ? 4 + (i16 >> 3) : (i16 >> 3) + (i16 & 4) / 2;
      zsel = (j >= 3 ? j - 2 : j + 1) * 4 | (2 * ((j >= 3 ? 1 : 0) + 2 * blk) + hh) << 8 | ((i16 & 1) || (i16 & 6) == 6 ? 8 << 16 : 0);
      asm volatile("" : "+{v[112:127]}"(cv), "+{v[64:71]}"(id), "+{v[128:129]}"(trv), "+v"(zsel));
    }
    if (w >= 5) asm volatile("s_waitcnt vmcnt(1)" ::: "memory"); else asm volatile("s_waitcnt vmcnt(0)" ::: "memory");
    asm volatile("" : "+v"(rec[0]), "+v"(rec[1]), "+v"(rec[2]), "+v"(rec[3]), "+v"(rec[4]), "+v"(rec[5]), "+v"(rec[6])::"memory");
    if (bad) nr = nw = 0;
    if (lane == 0) *(u32*)(lds + L_NW + w * 4) = nw;
  }
  while (true) {
    LANE_IDS;
    u32 rank[NS];
    u32 n4[NS], pa[NS], pa4[NS], ta16[NS];
    u32x4 slot[NS];
#pragma unroll
    for (int i = 0; i < NS; ++i) n4[i] = (rec[i] << 2) & 0xfcu;
    auto count = [&]() {
      asm volatile(
        "s_cmp_ge_u32 %[nf], 4\n\ts_cbranch_scc0 LG_%=\n\t"
        "ds_add_rtn_u32 %0, %7, %[one] offset:%c[o]\n\t"
        "ds_add_rtn_u32 %1, %8, %[one] offset:%c[o]\n\t"
        "ds_add_rtn_u32 %2, %9, %[one] offset:%c[o]\n\t"
        "ds_add_rtn_u32 %3, %10, %[one] offset:%c[o]\n\t"
        "s_cmp_gt_u32 %[nf], 4\n\ts_cselect_b64 exec, -1, %[pm]\n\ts_cbranch_execz LE_%=\n\tds_add_rtn_u32 %4, %11, %[one] offset:%c[o]\n\ts_cbranch_scc0 LE_%=\n\t"
        "s_cmp_gt_u32 %[nf], 5\n\ts_cselect_b64 exec, -1, %[pm]\n\ts_cbranch_execz LE_%=\n\tds_add_rtn_u32 %5, %12, %[one] offset:%c[o]\n\ts_cbranch_scc0 LE_%=\n\t"
        "s_cmp_gt_u32 %[nf], 6\n\ts_cselect_b64 exec, -1, %[pm]\n\ts_cbranch_execz LE_%=\n\tds_add_rtn_u32 %6, %13, %[one] offset:%c[o]\n\t"
        "s_branch LE_%=\n\tLG_%=:\n\t"
        "s_cmp_gt_u32 %[nf], 0\n\ts_cselect_b64 exec, -1, %[pm]\n\ts_cbranch_execz LE_%=\n\tds_add_rtn_u32 %0, %7, %[one] offset:%c[o]\n\ts_cbranch_scc0 LE_%=\n\t"
        "s_cmp_gt_u32 %[nf], 1\n\ts_cselect_b64 exec, -1, %[pm]\n\ts_cbranch_execz LE_%=\n\tds_add_rtn_u32 %1, %8, %[one] offset:%c[o]\n\ts_cbranch_scc0 LE_%=\n\t"
        "s_cmp_gt_u32 %[nf], 2\n\ts_cselect_b64 exec, -1, %[pm]\n\ts_cbranch_execz LE_%=\n\tds_add_rtn_u32 %2, %9, %[one] offset:%c[o]\n\ts_cbranch_scc0 LE_%=\n\t"
        "s_cmp_gt_u32 %[nf], 3\n\ts_cselect_b64 exec, -1, %[pm]\n\ts_cbranch_execz LE_%=\n\tds_add_rtn_u32 %3, %10, %[one] offset:%c[o]\n\t"
        "LE_%=:\n\ts_mov_b64 exec, -1"
        : "=&v"(rank[0]), "=&v"(rank[1]), "=&v"(rank[2]), "=&v"(rank[3]), "=&v"(rank[4]), "=&v"(rank[5]), "=&v"(rank[6])
        : "v"(n4[0]), "v"(n4[1]), "v"(n4[2]), "v"(n4[3]), "v"(n4[4]), "v"(n4[5]), "v"(n4[6]), [one] "v"(1), [o] "i"(L_CNT),
          [nf] "s"(nr >> 6), [pm] "s"((1ull << (nr & 63)) - 1)
        : "scc", "memory");
    };
    auto counted = [&]() {
      asm volatile("s_waitcnt lgkmcnt(0)\n\ts_barrier"
                   : "+v"(rank[0]), "+v"(rank[1]), "+v"(rank[2]), "+v"(rank[3]), "+v"(rank[4]), "+v"(rank[5]), "+v"(rank[6])::"memory");
    };
    count();
#pragma unroll
    for (int i = 0; i < NS; ++i) {
      if (i < 5 || nr > 64 * i) {
        const u32 sn = __builtin_amdgcn_ubfe(rec[i], 6, 14);
        pa[i] = (u32)__umul24(sn, 6u) + L_STG;
        pa4[i] = pa[i] & ~3u;
        ta16[i] = n4[i] * 4;
        slot[i].z = (u32)__umul24(off[i] >> 14, CH * 8) + (rec[i] >> 20) * 8;
        slot[i].w = sn | ((rec[i] << 27) & 0x38000000u);
        asm("" : "=v"(slot[i].x), "=v"(slot[i].y));
      } else {
        asm("" : "=v"(pa[i]), "=v"(pa4[i]), "=v"(ta16[i]), "=v"(slot[i]));
      }
      asm volatile("" : "+v"(pa[i]), "+v"(pa4[i]), "+v"(ta16[i]), "+v"(slot[i]));
    }
    counted();
    u32 cnt = *(const u32*)(lds + L_CNT + lane * 4);
    if (R0 == 0) {
      if (t == 0 && tid == 0) {
        const u32x2 g = {(taglo >> 24) | (taghi << 8), taghi >> 24};
        asm volatile("s_nop 4\n\tglobal_store_dwordx2 %0, %1, %2 sc1" ::"v"(WS_GEN), "v"(g), "s"(ws) : "memory");
      }
      if (w >= 3) touch(ws + WS_BT, (u32)min((t >> 3) * 313 + tid - 192, NN - 1) * 128, lb + L_SINK);
    }
    u32 incl = wave_scan_incl(cnt);
    if (__builtin_amdgcn_readlane(incl, 63) > CAP) {
      wcap = CAP / NW;
      nr = min(nr, wcap);
      lds_barrier();
      if (w == 0) *(u32*)(lds + L_CNT + lane * 4) = 0;
      lds_barrier();
      count();
      counted();
      cnt = *(const u32*)(lds + L_CNT + lane * 4);
      incl = wave_scan_incl(cnt);
    }
    const u32 excl = incl - cnt;
    const u32 T = __builtin_amdgcn_readlane(incl, 63);
    const u32 NT = (T + 31) >> 5;
    if (lane < 40) *(u32*)(lds + L_T + lane * 16 + 12) = excl;
    {
      u32 d0[NS], d1[NS];
      u32x4 pd[NS];
#pragma unroll
      for (int i = 0; i < NS; ++i) {
        if (i < 5 || nr > 64 * i) {
          d0[i] = *(const LAS u32*)(size_t)pa4[i];
          d1[i] = *(const LAS u32*)(size_t)(pa4[i] + 4);
          pd[i] = *(const LAS u32x4*)(size_t)(ta16[i] + L_T);
        }
      }
#pragma unroll
      for (int i = 0; i < NS; ++i) {
        if (i < 5 || nr > 64 * i) {
          const u32 xy = __builtin_amdgcn_alignbyte(d1[i], d0[i], pa[i]), z = __builtin_amdgcn_alignbyte(d1[i], d1[i], pa[i]);
          const float dx = __uint_as_float(pd[i].x) - bf_lo(xy), dy = __uint_as_float(pd[i].y) - bf_hi(xy);
          const float dz = __uint_as_float(pd[i].z) - bf_lo(z);
          const float d2 = __builtin_fmaf(dz, dz, __builtin_fmaf(dy, dy, dx * dx)) * NLOG2E;
          slot[i].x = pk_bf16(d2, dx);
          slot[i].y = pk_bf16(dy, dz);
          rank[i] = (pd[i].w + rank[i]) * 16;
        }
      }
      asm volatile(
        "s_cmp_ge_u32 %[nf], 4\n\ts_cbranch_scc0 LG_%=\n\t"
        "ds_write_b128 %0, %7 offset:%c[o]\n\t"
        "ds_write_b128 %1, %8 offset:%c[o]\n\t"
        "ds_write_b128 %2, %9 offset:%c[o]\n\t"
        "ds_write_b128 %3, %10 offset:%c[o]\n\t"
        "s_cmp_gt_u32 %[nf], 4\n\ts_cselect_b64 exec, -1, %[pm]\n\ts_cbranch_execz LE_%=\n\tds_write_b128 %4, %11 offset:%c[o]\n\ts_cbranch_scc0 LE_%=\n\t"
        "s_cmp_gt_u32 %[nf], 5\n\ts_cselect_b64 exec, -1, %[pm]\n\ts_cbranch_execz LE_%=\n\tds_write_b128 %5, %12 offset:%c[o]\n\ts_cbranch_scc0 LE_%=\n\t"
        "s_cmp_gt_u32 %[nf], 6\n\ts_cselect_b64 exec, -1, %[pm]\n\ts_cbranch_execz LE_%=\n\tds_write_b128 %6, %13 offset:%c[o]\n\t"
        "s_branch LE_%=\n\tLG_%=:\n\t"
        "s_cmp_gt_u32 %[nf], 0\n\ts_cselect_b64 exec, -1, %[pm]\n\ts_cbranch_execz LE_%=\n\tds_write_b128 %0, %7 offset:%c[o]\n\ts_cbranch_scc0 LE_%=\n\t"
        "s_cmp_gt_u32 %[nf], 1\n\ts_cselect_b64 exec, -1, %[pm]\n\ts_cbranch_execz LE_%=\n\tds_write_b128 %1, %8 offset:%c[o]\n\ts_cbranch_scc0 LE_%=\n\t"
        "s_cmp_gt_u32 %[nf], 2\n\ts_cselect_b64 exec, -1, %[pm]\n\ts_cbranch_execz LE_%=\n\tds_write_b128 %2, %9 offset:%c[o]\n\ts_cbranch_scc0 LE_%=\n\t"
        "s_cmp_gt_u32 %[nf], 3\n\ts_cselect_b64 exec, -1, %[pm]\n\ts_cbranch_execz LE_%=\n\tds_write_b128 %3, %10 offset:%c[o]\n\t"
        "LE_%=:\n\ts_mov_b64 exec, -1"
        :
        : "v"(rank[0]), "v"(rank[1]), "v"(rank[2]), "v"(rank[3]), "v"(rank[4]), "v"(rank[5]), "v"(rank[6]), "v"(slot[0]), "v"(slot[1]),
          "v"(slot[2]), "v"(slot[3]), "v"(slot[4]), "v"(slot[5]), "v"(slot[6]), [o] "i"(L_F),
          [nf] "s"(nr >> 6), [pm] "s"((1ull << (nr & 63)) - 1)
        : "scc", "memory");
    }
    if (w == 1 && T + lane < NT * 32) {
      *(u32x4*)(lds + L_F + (T + lane) * 16) = u32x4{0, 0, 0, 0};
    }
    const u32 tq = NT / NW, trm = NT % NW;
    const u32 tw0 = w * tq + min((u32)w, trm), tw1 = tw0 + tq + ((u32)w < trm ? 1 : 0);
    u32 cp = tw0 * 32;
    const u32 ep = min(tw1 * 32, T);
    auto aim = [&]() {
      LANE_IDS;
      cv[0] = lb + L_F + (cp >> 5) * 512 + r * 16;
      cv[1] = lb + L_F + (cp >> 5) * 512 + (lane >> 3) * 64 + 12;
      asm volatile("" : "+{v[112:127]}"(cv));
    };
    aim();
    lds_barrier();
    if (R0 == 0 && cp >= ep) {
      const u32x4 n0 = *(const u32x4*)(lds + L_NW), n1 = *(const u32x4*)(lds + L_NW + 16);
      nmax = __builtin_amdgcn_readfirstlane(max(max(max(n0.x, n0.y), max(n0.z, n0.w)), max(max(n1.x, n1.y), max(n1.z, n1.w))));
    }
    while (cp < ep) {
      const u32 stg = lb + L_STG + w * 4096;
      const u64 bta = (u64)(ws + WS_BT - 3072);
      const u32x4 srd = {(u32)bta, ((u32)(bta >> 32) & 0xffffu) | 128u << 16, 0xffffffffu, 0x00020000u};
      u32x2 q0;
      asm volatile(
          "ds_read_u16 v116, v113 offset:0\n\t"
          "ds_read_u16 v118, v113 offset:16\n\t"
          "ds_read_u16 v120, v113 offset:32\n\t"
          "ds_read_u16 v122, v113 offset:48\n\t"
          "ds_read_b32 v215, v112 offset:8\n\t"
          "s_mov_b32 m0, %[stg0]\n\t"
          "s_waitcnt lgkmcnt(1)\n\t"
          "buffer_load_dwordx4 v[116:117], %[srd], 0 idxen offen offset:0 sc1 lds\n\t"
          "buffer_load_dwordx4 v[118:119], %[srd], 0 idxen offen offset:1024 sc1 lds\n\t"
          "buffer_load_dwordx4 v[120:121], %[srd], 0 idxen offen offset:2048 sc1 lds\n\t"
          "buffer_load_dwordx4 v[122:123], %[srd], 0 idxen offen offset:3072 sc1 lds\n\t"
          "s_waitcnt lgkmcnt(0)\n\t"
          "s_mov_b32 exec_hi, 0\n\t"
          "global_load_dwordx2 v[178:179], v215, %[eb]\n\t"
          "s_mov_b32 exec_hi, -1\n\t"
          : "+{v[112:127]}"(cv), "={v[178:179]}"(q0)
          : [stg0] "s"(stg), [srd] "s"(srd), [eb] "s"(ea)
          : "v215", "memory");
      LANE_IDS;
      if (R0 == 0) {
        const u32x4 n0 = *(const u32x4*)(lds + L_NW), n1 = *(const u32x4*)(lds + L_NW + 16);
        nmax = __builtin_amdgcn_readfirstlane(max(max(max(n0.x, n0.y), max(n0.z, n0.w)), max(max(n1.x, n1.y), max(n1.z, n1.w))));
      }
      const bool slow = nmax > wcap || __builtin_amdgcn_ballot_w64(cnt >= tq * 32 && cnt > 0) != 0;
      u32 rem, mk, mlast, fa[4], fz;
      u32x8 bw;
      const u32 nb = min((u32)__builtin_popcountll(__builtin_amdgcn_ballot_w64(incl <= cp)), 39u);
      const u32 rp = min(ep, (u32)__builtin_amdgcn_readlane(excl, nb + 8));
      const u32 ta = cp >> 5;
      rem = ((rp + 31) >> 5) - ta;
      mk = ~0u << (cp & 31);
      mlast = ~0u >> ((0u - rp) & 31);
      const u32 nlast = __builtin_popcountll(__builtin_amdgcn_ballot_w64(incl < rp));
      cp = rp;
      const u32 a0 = lb + L_WIMG + (4 * hh + qq) * 128 + (16 * blk + 4 * pp) * 2;
      const u32 a1 = lb + L_WIMG + (8 + nb + ((4 * hh + qq - nb) & 7)) * 128 + (16 * blk + 4 * pp) * 2;
      const u32x2 u0 = tr_read(a0), u1 = tr_read(a1), u2 = tr_read(a0 + 64), u3 = tr_read(a1 + 64);
      bw = u32x8{u0.x, u0.y, u1.x, u1.y, u2.x, u2.y, u3.x, u3.y};
      const u32 idle = L_STG + w * 4096 + lane * 4, span = nlast - nb, row0 = (w & 1) * 40 + nb;
#pragma unroll
      for (int g = 0; g < 4; ++g) {
        const u32 k = (hh + (2 * g - nb)) & 7, x = (u32)__umul24(k, 272u) + (L_MACC + row0 * 272 + r * 4);
        fa[g] = k <= span ? x : idle;
      }
      {
        const u32 k = ((zsel >> 8) - nb) & 7, x = k * 16 + (L_ZACC + row0 * 16) + (zsel & 0xffu);
        fz = k + (zsel >> 16) <= span ? x : idle;
      }
      asm volatile("" : "+v"(fa[0]), "+v"(fa[1]), "+v"(fa[2]), "+v"(fa[3]), "+v"(fz));
      f32x16 y0, y1;
      u32 tmp;
      if (w >= 4) __builtin_amdgcn_s_setprio(1);
      asm volatile(
          "ds_read_u8 v212, v112 offset:15\n\t"
          "s_mov_b32 m0, %[stg0]\n\t"
          "s_cmp_eq_u32 %[rem], 1\n\t"
          "s_cselect_b32 %[tmp], %[mlast], -1\n\t"
          "s_and_b32 %[mk], %[mk], %[tmp]\n\t"
          "s_waitcnt lgkmcnt(0)\n\t"
          "v_add_u32 v214, v212, v115\n\t"
          "v_xor_b32 v213, v212, v114\n\t"
          "ds_read_b64 v[180:181], v214\n\t"
          "s_mov_b32 exec_lo, 0\n\t"
          "ds_read_b64 v[178:179], v112\n\t"
          "s_mov_b32 exec_lo, -1\n\t"
          "ds_read_u16 v116, v113 offset:512\n\t"
          "ds_read_u16 v118, v113 offset:528\n\t"
          "ds_read_u16 v120, v113 offset:544\n\t"
          "ds_read_u16 v122, v113 offset:560\n\t"
          "ds_read_b32 v215, v112 offset:520\n\t"
          "v_mov_b32 v211, 0\n\t"
          "v_mov_b32 v216, 0x3f80\n\t"
          "v_mov_b64 v[80:81], 0\n\t"
          "v_mov_b64 v[82:83], 0\n\t"
          "v_mov_b64 v[84:85], 0\n\t"
          "v_mov_b64 v[86:87], 0\n\t"
          "v_mov_b64 v[88:89], 0\n\t"
          "v_mov_b64 v[90:91], 0\n\t"
          "v_mov_b64 v[92:93], 0\n\t"
          "v_mov_b64 v[94:95], 0\n\t"
          "v_mov_b64 v[96:97], 0\n\t"
          "v_mov_b64 v[98:99], 0\n\t"
          "v_mov_b64 v[100:101], 0\n\t"
          "v_mov_b64 v[102:103], 0\n\t"
          "v_mov_b64 v[104:105], 0\n\t"
          "v_mov_b64 v[106:107], 0\n\t"
          "v_mov_b64 v[108:109], 0\n\t"
          "v_mov_b64 v[110:111], 0\n\t"
          "v_mov_b64 v[186:187], 0\n\t"
          "v_mov_b64 v[188:189], 0\n\t"
          "v_mov_b64 v[190:191], 0\n\t"
          "v_mov_b64 v[192:193], 0\n\t"
          "v_mov_b64 v[202:203], 0\n\t"
          "v_mov_b64 v[204:205], 0\n\t"
          "v_mov_b64 v[206:207], 0\n\t"
          "v_mov_b64 v[208:209], 0\n\t"
          "s_waitcnt lgkmcnt(5)\n\t"
          "v_and_or_b32 v210, v178, %[chi], v216\n\t"
          "s_mov_b32 exec_lo, 0\n\t"
          "s_mov_b32 exec_hi, %[mk]\n\t"
          "ds_write2_b32 v213, v210, v179 offset1:1\n\t"
          "s_mov_b64 exec, -1\n\t"
          "s_cmp_eq_u32 %[rem], 1\n\t"
          "s_waitcnt vmcnt(1)\n\t"
          "ds_read_b128 v[162:165], v124\n\t"
          "ds_read_b128 v[166:169], v125\n\t"
          "ds_read_b128 v[170:173], v126\n\t"
          "ds_read_b128 v[174:177], v127\n\t"
          "s_waitcnt lgkmcnt(0)\n\t"
          "s_cbranch_scc1 LP1_%=\n\t"
          "buffer_load_dwordx4 v[116:117], %[srd], 0 idxen offen offset:0 sc1 lds\n\t"
          "buffer_load_dwordx4 v[118:119], %[srd], 0 idxen offen offset:1024 sc1 lds\n\t"
          "buffer_load_dwordx4 v[120:121], %[srd], 0 idxen offen offset:2048 sc1 lds\n\t"
          "buffer_load_dwordx4 v[122:123], %[srd], 0 idxen offen offset:3072 sc1 lds\n\t"
          "s_mov_b32 exec_hi, 0\n\t"
          "global_load_dwordx2 v[182:183], v215, %[eb]\n\t"
          "s_mov_b32 exec_hi, -1\n\t"
          "v_mfma_f32_32x32x16_bf16 v[130:145], v[162:165], v[64:67], 0\n\t"
          "v_mfma_f32_32x32x16_bf16 v[130:145], v[166:169], v[68:71], v[130:145]\n\t"
          "s_waitcnt vmcnt(5)\n\t"
          "s_branch LP2_%=\n\t"
          "LP1_%=:\n\t"
          "v_mfma_f32_32x32x16_bf16 v[130:145], v[162:165], v[64:67], 0\n\t"
          "v_mfma_f32_32x32x16_bf16 v[130:145], v[166:169], v[68:71], v[130:145]\n\t"
          "s_waitcnt vmcnt(0)\n\t"
          "LP2_%=:\n\t"
          "v_mfma_f32_32x32x16_bf16 v[130:145], v[178:181], v[72:75], v[130:145]\n\t"
          "s_nop 10\n\t"
          "LA0_%=:\n\t"
          "v_mfma_f32_32x32x16_bf16 v[96:111], v[186:189], v[202:205], v[96:111]\n\t"
          "v_exp_f32 v194, v130\n\t"
          "v_exp_f32 v195, v131\n\t"
          "v_exp_f32 v196, v132\n\t"
          "v_exp_f32 v197, v133\n\t"
          "v_mfma_f32_32x32x16_bf16 v[96:111], v[190:193], v[206:209], v[96:111]\n\t"
          "v_exp_f32 v198, v134\n\t"
          "v_exp_f32 v199, v135\n\t"
          "v_exp_f32 v200, v136\n\t"
          "v_exp_f32 v201, v137\n\t"
          "ds_read_b64_tr_b16 v[186:187], v128\n\t"
          "ds_read_b64_tr_b16 v[188:189], v129 offset:512\n\t"
          "ds_read_b64_tr_b16 v[190:191], v128 offset:1024\n\t"
          "ds_read_b64_tr_b16 v[192:193], v129 offset:1536\n\t"
          "v_pk_add_f32 v[194:195], v[194:195], 1.0 op_sel_hi:[1,0]\n\t"
          "v_pk_add_f32 v[196:197], v[196:197], 1.0 op_sel_hi:[1,0]\n\t"
          "v_pk_add_f32 v[198:199], v[198:199], 1.0 op_sel_hi:[1,0]\n\t"
          "v_pk_add_f32 v[200:201], v[200:201], 1.0 op_sel_hi:[1,0]\n\t"
          "v_rcp_f32 v194, v194\n\t"
          "v_rcp_f32 v195, v195\n\t"
          "v_rcp_f32 v196, v196\n\t"
          "v_rcp_f32 v197, v197\n\t"
          "v_rcp_f32 v198, v198\n\t"
          "v_rcp_f32 v199, v199\n\t"
          "v_rcp_f32 v200, v200\n\t"
          "v_rcp_f32 v201, v201\n\t"
          "ds_read_u16 v116, v113 offset:1024\n\t"
          "ds_read_u16 v118, v113 offset:1040\n\t"
          "ds_read_u16 v120, v113 offset:1056\n\t"
          "ds_read_u16 v122, v113 offset:1072\n\t"
          "v_pk_mul_f32 v[194:195], v[194:195], v[130:131]\n\t"
          "v_pk_mul_f32 v[196:197], v[196:197], v[132:133]\n\t"
          "v_pk_mul_f32 v[198:199], v[198:199], v[134:135]\n\t"
          "v_pk_mul_f32 v[200:201], v[200:201], v[136:137]\n\t"
          "v_cvt_pk_bf16_f32 v202, v194, v195\n\t"
          "v_cvt_pk_bf16_f32 v203, v196, v197\n\t"
          "v_cvt_pk_bf16_f32 v204, v198, v199\n\t"
          "v_cvt_pk_bf16_f32 v205, v200, v201\n\t"
          "v_mfma_f32_32x32x16_bf16 v[146:161], v[170:173], v[64:67], 0\n\t"
          "v_exp_f32 v194, v138\n\t"
          "v_exp_f32 v195, v139\n\t"
          "v_exp_f32 v196, v140\n\t"
          "v_exp_f32 v197, v141\n\t"
          "v_mfma_f32_32x32x16_bf16 v[146:161], v[174:177], v[68:71], v[146:161]\n\t"
          "v_exp_f32 v198, v142\n\t"
          "v_exp_f32 v199, v143\n\t"
          "v_exp_f32 v200, v144\n\t"
          "v_exp_f32 v201, v145\n\t"
          "ds_read_b32 v215, v112 offset:1032\n\t"
          "ds_read_u8 v212, v112 offset:527\n\t"
          "v_pk_add_f32 v[194:195], v[194:195], 1.0 op_sel_hi:[1,0]\n\t"
          "v_pk_add_f32 v[196:197], v[196:197], 1.0 op_sel_hi:[1,0]\n\t"
          "v_pk_add_f32 v[198:199], v[198:199], 1.0 op_sel_hi:[1,0]\n\t"
          "v_pk_add_f32 v[200:201], v[200:201], 1.0 op_sel_hi:[1,0]\n\t"
          "v_mfma_f32_32x32x16_bf16 v[146:161], v[178:181], v[76:79], v[146:161]\n\t"
          "v_rcp_f32 v194, v194\n\t"
          "v_rcp_f32 v195, v195\n\t"
          "v_rcp_f32 v196, v196\n\t"
          "v_rcp_f32 v197, v197\n\t"
          "v_rcp_f32 v198, v198\n\t"
          "v_rcp_f32 v199, v199\n\t"
          "v_rcp_f32 v200, v200\n\t"
          "v_rcp_f32 v201, v201\n\t"
          "v_pk_mul_f32 v[194:195], v[194:195], v[138:139]\n\t"
          "v_pk_mul_f32 v[196:197], v[196:197], v[140:141]\n\t"
          "v_pk_mul_f32 v[198:199], v[198:199], v[142:143]\n\t"
          "v_pk_mul_f32 v[200:201], v[200:201], v[144:145]\n\t"
          "v_cvt_pk_bf16_f32 v206, v194, v195\n\t"
          "v_cvt_pk_bf16_f32 v207, v196, v197\n\t"
          "v_cvt_pk_bf16_f32 v208, v198, v199\n\t"
          "v_cvt_pk_bf16_f32 v209, v200, v201\n\t"
          "s_cmp_le_u32 %[rem], 2\n\t"
          "s_cbranch_scc1 LT0_%=\n\t"
          "s_waitcnt vmcnt(1) lgkmcnt(0)\n\t"
          "ds_read_b128 v[162:165], v124\n\t"
          "ds_read_b128 v[166:169], v125\n\t"
          "ds_read_b128 v[170:173], v126\n\t"
          "ds_read_b128 v[174:177], v127\n\t"
          "s_mov_b32 exec_lo, 0\n\t"
          "ds_write2_b32 v213, v211, v211 offset1:1\n\t"
          "ds_read_b64 v[182:183], v112 offset:512\n\t"
          "s_mov_b32 exec_lo, -1\n\t"
          "v_add_u32 v214, v212, v115\n\t"
          "v_xor_b32 v213, v212, v114\n\t"
          "ds_read_b64 v[184:185], v214\n\t"
          "v_mfma_f32_32x32x16_bf16 v[80:95], v[186:189], v[202:205], v[80:95]\n\t"
          "v_exp_f32 v194, v146\n\t"
          "v_exp_f32 v195, v147\n\t"
          "v_exp_f32 v196, v148\n\t"
          "v_exp_f32 v197, v149\n\t"
          "v_mfma_f32_32x32x16_bf16 v[80:95], v[190:193], v[206:209], v[80:95]\n\t"
          "v_exp_f32 v198, v150\n\t"
          "v_exp_f32 v199, v151\n\t"
          "v_exp_f32 v200, v152\n\t"
          "v_exp_f32 v201, v153\n\t"
          "s_waitcnt lgkmcnt(3)\n\t"
          "buffer_load_dwordx4 v[116:117], %[srd], 0 idxen offen offset:0 sc1 lds\n\t"
          "buffer_load_dwordx4 v[118:119], %[srd], 0 idxen offen offset:1024 sc1 lds\n\t"
          "v_pk_add_f32 v[194:195], v[194:195], 1.0 op_sel_hi:[1,0]\n\t"
          "v_pk_add_f32 v[196:197], v[196:197], 1.0 op_sel_hi:[1,0]\n\t"
          "v_pk_add_f32 v[198:199], v[198:199], 1.0 op_sel_hi:[1,0]\n\t"
          "v_pk_add_f32 v[200:201], v[200:201], 1.0 op_sel_hi:[1,0]\n\t"
          "v_rcp_f32 v194, v194\n\t"
          "v_rcp_f32 v195, v195\n\t"
          "v_rcp_f32 v196, v196\n\t"
          "v_rcp_f32 v197, v197\n\t"
          "v_rcp_f32 v198, v198\n\t"
          "v_rcp_f32 v199, v199\n\t"
          "v_rcp_f32 v200, v200\n\t"
          "v_rcp_f32 v201, v201\n\t"
          "buffer_load_dwordx4 v[120:121], %[srd], 0 idxen offen offset:2048 sc1 lds\n\t"
          "buffer_load_dwordx4 v[122:123], %[srd], 0 idxen offen offset:3072 sc1 lds\n\t"
          "v_pk_mul_f32 v[194:195], v[194:195], v[146:147]\n\t"
          "v_pk_mul_f32 v[196:197], v[196:197], v[148:149]\n\t"
          "v_pk_mul_f32 v[198:199], v[198:199], v[150:151]\n\t"
          "v_pk_mul_f32 v[200:201], v[200:201], v[152:153]\n\t"
          "v_cvt_pk_bf16_f32 v202, v194, v195\n\t"
          "v_cvt_pk_bf16_f32 v203, v196, v197\n\t"
          "v_cvt_pk_bf16_f32 v204, v198, v199\n\t"
          "v_cvt_pk_bf16_f32 v205, v200, v201\n\t"
          "s_sub_u32 %[rem], %[rem], 1\n\t"
          "s_cmp_eq_u32 %[rem], 3\n\t"
          "s_cbranch_scc0 LS0_%=\n\t"
          "s_setprio 0\n\t"
          "LS0_%=:\n\t"
          "v_mfma_f32_32x32x16_bf16 v[130:145], v[162:165], v[64:67], 0\n\t"
          "v_exp_f32 v194, v154\n\t"
          "v_exp_f32 v195, v155\n\t"
          "v_exp_f32 v196, v156\n\t"
          "v_exp_f32 v197, v157\n\t"
          "v_mfma_f32_32x32x16_bf16 v[130:145], v[166:169], v[68:71], v[130:145]\n\t"
          "v_exp_f32 v198, v158\n\t"
          "v_exp_f32 v199, v159\n\t"
          "v_exp_f32 v200, v160\n\t"
          "v_exp_f32 v201, v161\n\t"
          "v_pk_add_f32 v[194:195], v[194:195], 1.0 op_sel_hi:[1,0]\n\t"
          "v_pk_add_f32 v[196:197], v[196:197], 1.0 op_sel_hi:[1,0]\n\t"
          "v_pk_add_f32 v[198:199], v[198:199], 1.0 op_sel_hi:[1,0]\n\t"
          "v_pk_add_f32 v[200:201], v[200:201], 1.0 op_sel_hi:[1,0]\n\t"
          "s_waitcnt vmcnt(4) lgkmcnt(0)\n\t"
          "v_mfma_f32_32x32x16_bf16 v[130:145], v[182:185], v[72:75], v[130:145]\n\t"
          "v_rcp_f32 v194, v194\n\t"
          "v_rcp_f32 v195, v195\n\t"
          "v_rcp_f32 v196, v196\n\t"
          "v_rcp_f32 v197, v197\n\t"
          "v_rcp_f32 v198, v198\n\t"
          "v_rcp_f32 v199, v199\n\t"
          "v_rcp_f32 v200, v200\n\t"
          "v_rcp_f32 v201, v201\n\t"
          "v_and_or_b32 v210, v182, %[chi], v216\n\t"
          "s_mov_b32 exec_hi, 0\n\t"
          "global_load_dwordx2 v[178:179], v215, %[eb]\n\t"
          "s_not_b64 exec, exec\n\t"
          "ds_write2_b32 v213, v210, v183 offset1:1\n\t"
          "s_mov_b32 exec_lo, -1\n\t"
          "v_pk_mul_f32 v[194:195], v[194:195], v[154:155]\n\t"
          "v_pk_mul_f32 v[196:197], v[196:197], v[156:157]\n\t"
          "v_pk_mul_f32 v[198:199], v[198:199], v[158:159]\n\t"
          "v_pk_mul_f32 v[200:201], v[200:201], v[160:161]\n\t"
          "v_cvt_pk_bf16_f32 v206, v194, v195\n\t"
          "v_cvt_pk_bf16_f32 v207, v196, v197\n\t"
          "v_cvt_pk_bf16_f32 v208, v198, v199\n\t"
          "v_cvt_pk_bf16_f32 v209, v200, v201\n\t"
          "LA1_%=:\n\t"
          "v_mfma_f32_32x32x16_bf16 v[96:111], v[186:189], v[202:205], v[96:111]\n\t"
          "v_exp_f32 v194, v130\n\t"
          "v_exp_f32 v195, v131\n\t"
          "v_exp_f32 v196, v132\n\t"
          "v_exp_f32 v197, v133\n\t"
          "v_mfma_f32_32x32x16_bf16 v[96:111], v[190:193], v[206:209], v[96:111]\n\t"
          "v_exp_f32 v198, v134\n\t"
          "v_exp_f32 v199, v135\n\t"
          "v_exp_f32 v200, v136\n\t"
          "v_exp_f32 v201, v137\n\t"
          "ds_read_b64_tr_b16 v[186:187], v128\n\t"
          "ds_read_b64_tr_b16 v[188:189], v129 offset:512\n\t"
          "ds_read_b64_tr_b16 v[190:191], v128 offset:1024\n\t"
          "ds_read_b64_tr_b16 v[192:193], v129 offset:1536\n\t"
          "v_pk_add_f32 v[194:195], v[194:195], 1.0 op_sel_hi:[1,0]\n\t"
          "v_pk_add_f32 v[196:197], v[196:197], 1.0 op_sel_hi:[1,0]\n\t"
          "v_pk_add_f32 v[198:199], v[198:199], 1.0 op_sel_hi:[1,0]\n\t"
          "v_pk_add_f32 v[200:201], v[200:201], 1.0 op_sel_hi:[1,0]\n\t"
          "v_rcp_f32 v194, v194\n\t"
          "v_rcp_f32 v195, v195\n\t"
          "v_rcp_f32 v196, v196\n\t"
          "v_rcp_f32 v197, v197\n\t"
          "v_rcp_f32 v198, v198\n\t"
          "v_rcp_f32 v199, v199\n\t"
          "v_rcp_f32 v200, v200\n\t"
          "v_rcp_f32 v201, v201\n\t"
          "ds_read_u16 v116, v113 offset:1536\n\t"
          "ds_read_u16 v118, v113 offset:1552\n\t"
          "ds_read_u16 v120, v113 offset:1568\n\t"
          "ds_read_u16 v122, v113 offset:1584\n\t"
          "v_pk_mul_f32 v[194:195], v[194:195], v[130:131]\n\t"
          "v_pk_mul_f32 v[196:197], v[196:197], v[132:133]\n\t"
          "v_pk_mul_f32 v[198:199], v[198:199], v[134:135]\n\t"
          "v_pk_mul_f32 v[200:201], v[200:201], v[136:137]\n\t"
          "v_cvt_pk_bf16_f32 v202, v194, v195\n\t"
          "v_cvt_pk_bf16_f32 v203, v196, v197\n\t"
          "v_cvt_pk_bf16_f32 v204, v198, v199\n\t"
          "v_cvt_pk_bf16_f32 v205, v200, v201\n\t"
          "v_mfma_f32_32x32x16_bf16 v[146:161], v[170:173], v[64:67], 0\n\t"
          "v_exp_f32 v194, v138\n\t"
          "v_exp_f32 v195, v139\n\t"
          "v_exp_f32 v196, v140\n\t"
          "v_exp_f32 v197, v141\n\t"
          "v_mfma_f32_32x32x16_bf16 v[146:161], v[174:177], v[68:71], v[146:161]\n\t"
          "v_exp_f32 v198, v142\n\t"
          "v_exp_f32 v199, v143\n\t"
          "v_exp_f32 v200, v144\n\t"
          "v_exp_f32 v201, v145\n\t"
          "ds_read_b32 v215, v112 offset:1544\n\t"
          "ds_read_u8 v212, v112 offset:1039\n\t"
          "v_pk_add_f32 v[194:195], v[194:195], 1.0 op_sel_hi:[1,0]\n\t"
          "v_pk_add_f32 v[196:197], v[196:197], 1.0 op_sel_hi:[1,0]\n\t"
          "v_pk_add_f32 v[198:199], v[198:199], 1.0 op_sel_hi:[1,0]\n\t"
          "v_pk_add_f32 v[200:201], v[200:201], 1.0 op_sel_hi:[1,0]\n\t"
          "v_mfma_f32_32x32x16_bf16 v[146:161], v[182:185], v[76:79], v[146:161]\n\t"
          "v_rcp_f32 v194, v194\n\t"
          "v_rcp_f32 v195, v195\n\t"
          "v_rcp_f32 v196, v196\n\t"
          "v_rcp_f32 v197, v197\n\t"
          "v_rcp_f32 v198, v198\n\t"
          "v_rcp_f32 v199, v199\n\t"
          "v_rcp_f32 v200, v200\n\t"
          "v_rcp_f32 v201, v201\n\t"
          "v_pk_mul_f32 v[194:195], v[194:195], v[138:139]\n\t"
          "v_pk_mul_f32 v[196:197], v[196:197], v[140:141]\n\t"
          "v_pk_mul_f32 v[198:199], v[198:199], v[142:143]\n\t"
          "v_pk_mul_f32 v[200:201], v[200:201], v[144:145]\n\t"
          "v_cvt_pk_bf16_f32 v206, v194, v195\n\t"
          "v_cvt_pk_bf16_f32 v207, v196, v197\n\t"
          "v_cvt_pk_bf16_f32 v208, v198, v199\n\t"
          "v_cvt_pk_bf16_f32 v209, v200, v201\n\t"
          "s_cmp_le_u32 %[rem], 2\n\t"
          "s_cbranch_scc1 LT1_%=\n\t"
          "s_waitcnt vmcnt(1) lgkmcnt(0)\n\t"
          "ds_read_b128 v[162:165], v124\n\t"
          "ds_read_b128 v[166:169], v125\n\t"
          "ds_read_b128 v[170:173], v126\n\t"
          "ds_read_b128 v[174:177], v127\n\t"
          "s_mov_b32 exec_lo, 0\n\t"
          "ds_write2_b32 v213, v211, v211 offset1:1\n\t"
          "ds_read_b64 v[178:179], v112 offset:1024\n\t"
          "s_mov_b32 exec_lo, -1\n\t"
          "v_add_u32 v214, v212, v115\n\t"
          "v_xor_b32 v213, v212, v114\n\t"
          "ds_read_b64 v[180:181], v214\n\t"
          "v_mfma_f32_32x32x16_bf16 v[80:95], v[186:189], v[202:205], v[80:95]\n\t"
          "v_exp_f32 v194, v146\n\t"
          "v_exp_f32 v195, v147\n\t"
          "v_exp_f32 v196, v148\n\t"
          "v_exp_f32 v197, v149\n\t"
          "v_mfma_f32_32x32x16_bf16 v[80:95], v[190:193], v[206:209], v[80:95]\n\t"
          "v_exp_f32 v198, v150\n\t"
          "v_exp_f32 v199, v151\n\t"
          "v_exp_f32 v200, v152\n\t"
          "v_exp_f32 v201, v153\n\t"
          "s_waitcnt lgkmcnt(3)\n\t"
          "buffer_load_dwordx4 v[116:117], %[srd], 0 idxen offen offset:0 sc1 lds\n\t"
          "buffer_load_dwordx4 v[118:119], %[srd], 0 idxen offen offset:1024 sc1 lds\n\t"
          "v_pk_add_f32 v[194:195], v[194:195], 1.0 op_sel_hi:[1,0]\n\t"
          "v_pk_add_f32 v[196:197], v[196:197], 1.0 op_sel_hi:[1,0]\n\t"
          "v_pk_add_f32 v[198:199], v[198:199], 1.0 op_sel_hi:[1,0]\n\t"
          "v_pk_add_f32 v[200:201], v[200:201], 1.0 op_sel_hi:[1,0]\n\t"
          "v_rcp_f32 v194, v194\n\t"
          "v_rcp_f32 v195, v195\n\t"
          "v_rcp_f32 v196, v196\n\t"
          "v_rcp_f32 v197, v197\n\t"
          "v_rcp_f32 v198, v198\n\t"
          "v_rcp_f32 v199, v199\n\t"
          "v_rcp_f32 v200, v200\n\t"
          "v_rcp_f32 v201, v201\n\t"
          "buffer_load_dwordx4 v[120:121], %[srd], 0 idxen offen offset:2048 sc1 lds\n\t"
          "buffer_load_dwordx4 v[122:123], %[srd], 0 idxen offen offset:3072 sc1 lds\n\t"
          "v_pk_mul_f32 v[194:195], v[194:195], v[146:147]\n\t"
          "v_pk_mul_f32 v[196:197], v[196:197], v[148:149]\n\t"
          "v_pk_mul_f32 v[198:199], v[198:199], v[150:151]\n\t"
          "v_pk_mul_f32 v[200:201], v[200:201], v[152:153]\n\t"
          "v_cvt_pk_bf16_f32 v202, v194, v195\n\t"
          "v_cvt_pk_bf16_f32 v203, v196, v197\n\t"
          "v_cvt_pk_bf16_f32 v204, v198, v199\n\t"
          "v_cvt_pk_bf16_f32 v205, v200, v201\n\t"
          "s_sub_u32 %[rem], %[rem], 1\n\t"
          "s_cmp_eq_u32 %[rem], 3\n\t"
          "s_cbranch_scc0 LS1_%=\n\t"
          "s_setprio 0\n\t"
          "LS1_%=:\n\t"
          "v_add_u32 v112, 0x400, v112\n\t"
          "v_add_u32 v113, 0x400, v113\n\t"
          "v_mfma_f32_32x32x16_bf16 v[130:145], v[162:165], v[64:67], 0\n\t"
          "v_exp_f32 v194, v154\n\t"
          "v_exp_f32 v195, v155\n\t"
          "v_exp_f32 v196, v156\n\t"
          "v_exp_f32 v197, v157\n\t"
          "v_mfma_f32_32x32x16_bf16 v[130:145], v[166:169], v[68:71], v[130:145]\n\t"
          "v_exp_f32 v198, v158\n\t"
          "v_exp_f32 v199, v159\n\t"
          "v_exp_f32 v200, v160\n\t"
          "v_exp_f32 v201, v161\n\t"
          "v_pk_add_f32 v[194:195], v[194:195], 1.0 op_sel_hi:[1,0]\n\t"
          "v_pk_add_f32 v[196:197], v[196:197], 1.0 op_sel_hi:[1,0]\n\t"
          "v_pk_add_f32 v[198:199], v[198:199], 1.0 op_sel_hi:[1,0]\n\t"
          "v_pk_add_f32 v[200:201], v[200:201], 1.0 op_sel_hi:[1,0]\n\t"
          "s_waitcnt vmcnt(4) lgkmcnt(0)\n\t"
          "v_mfma_f32_32x32x16_bf16 v[130:145], v[178:181], v[72:75], v[130:145]\n\t"
          "v_rcp_f32 v194, v194\n\t"
          "v_rcp_f32 v195, v195\n\t"
          "v_rcp_f32 v196, v196\n\t"
          "v_rcp_f32 v197, v197\n\t"
          "v_rcp_f32 v198, v198\n\t"
          "v_rcp_f32 v199, v199\n\t"
          "v_rcp_f32 v200, v200\n\t"
          "v_rcp_f32 v201, v201\n\t"
          "v_and_or_b32 v210, v178, %[chi], v216\n\t"
          "s_mov_b32 exec_hi, 0\n\t"
          "global_load_dwordx2 v[182:183], v215, %[eb]\n\t"
          "s_not_b64 exec, exec\n\t"
          "ds_write2_b32 v213, v210, v179 offset1:1\n\t"
          "s_mov_b32 exec_lo, -1\n\t"
          "v_pk_mul_f32 v[194:195], v[194:195], v[154:155]\n\t"
          "v_pk_mul_f32 v[196:197], v[196:197], v[156:157]\n\t"
          "v_pk_mul_f32 v[198:199], v[198:199], v[158:159]\n\t"
          "v_pk_mul_f32 v[200:201], v[200:201], v[160:161]\n\t"
          "v_cvt_pk_bf16_f32 v206, v194, v195\n\t"
          "v_cvt_pk_bf16_f32 v207, v196, v197\n\t"
          "v_cvt_pk_bf16_f32 v208, v198, v199\n\t"
          "v_cvt_pk_bf16_f32 v209, v200, v201\n\t"
          "s_branch LA0_%=\n\t"
          "LT0_%=:\n\t"
          "s_setprio 0\n\t"
          "s_cmp_eq_u32 %[rem], 1\n\t"
          "s_cbranch_scc1 LLAST_%=\n\t"
          "s_waitcnt vmcnt(1) lgkmcnt(0)\n\t"
          "ds_read_b128 v[162:165], v124\n\t"
          "ds_read_b128 v[166:169], v125\n\t"
          "ds_read_b128 v[170:173], v126\n\t"
          "ds_read_b128 v[174:177], v127\n\t"
          "s_mov_b32 exec_lo, 0\n\t"
          "ds_write2_b32 v213, v211, v211 offset1:1\n\t"
          "ds_read_b64 v[182:183], v112 offset:512\n\t"
          "s_mov_b32 exec_lo, -1\n\t"
          "v_add_u32 v214, v212, v115\n\t"
          "v_xor_b32 v213, v212, v114\n\t"
          "ds_read_b64 v[184:185], v214\n\t"
          "v_mfma_f32_32x32x16_bf16 v[80:95], v[186:189], v[202:205], v[80:95]\n\t"
          "v_exp_f32 v194, v146\n\t"
          "v_exp_f32 v195, v147\n\t"
          "v_exp_f32 v196, v148\n\t"
          "v_exp_f32 v197, v149\n\t"
          "v_mfma_f32_32x32x16_bf16 v[80:95], v[190:193], v[206:209], v[80:95]\n\t"
          "v_exp_f32 v198, v150\n\t"
          "v_exp_f32 v199, v151\n\t"
          "v_exp_f32 v200, v152\n\t"
          "v_exp_f32 v201, v153\n\t"
          "s_waitcnt lgkmcnt(3)\n\t"
          "v_pk_add_f32 v[194:195], v[194:195], 1.0 op_sel_hi:[1,0]\n\t"
          "v_pk_add_f32 v[196:197], v[196:197], 1.0 op_sel_hi:[1,0]\n\t"
          "v_pk_add_f32 v[198:199], v[198:199], 1.0 op_sel_hi:[1,0]\n\t"
          "v_pk_add_f32 v[200:201], v[200:201], 1.0 op_sel_hi:[1,0]\n\t"
          "v_rcp_f32 v194, v194\n\t"
          "v_rcp_f32 v195, v195\n\t"
          "v_rcp_f32 v196, v196\n\t"
          "v_rcp_f32 v197, v197\n\t"
          "v_rcp_f32 v198, v198\n\t"
          "v_rcp_f32 v199, v199\n\t"
          "v_rcp_f32 v200, v200\n\t"
          "v_rcp_f32 v201, v201\n\t"
          "v_pk_mul_f32 v[194:195], v[194:195], v[146:147]\n\t"
          "v_pk_mul_f32 v[196:197], v[196:197], v[148:149]\n\t"
          "v_pk_mul_f32 v[198:199], v[198:199], v[150:151]\n\t"
          "v_pk_mul_f32 v[200:201], v[200:201], v[152:153]\n\t"
          "v_cvt_pk_bf16_f32 v202, v194, v195\n\t"
          "v_cvt_pk_bf16_f32 v203, v196, v197\n\t"
          "v_cvt_pk_bf16_f32 v204, v198, v199\n\t"
          "v_cvt_pk_bf16_f32 v205, v200, v201\n\t"
          "s_sub_u32 %[rem], %[rem], 1\n\t"
          "v_mfma_f32_32x32x16_bf16 v[130:145], v[162:165], v[64:67], 0\n\t"
          "v_exp_f32 v194, v154\n\t"
          "v_exp_f32 v195, v155\n\t"
          "v_exp_f32 v196, v156\n\t"
          "v_exp_f32 v197, v157\n\t"
          "v_mfma_f32_32x32x16_bf16 v[130:145], v[166:169], v[68:71], v[130:145]\n\t"
          "v_exp_f32 v198, v158\n\t"
          "v_exp_f32 v199, v159\n\t"
          "v_exp_f32 v200, v160\n\t"
          "v_exp_f32 v201, v161\n\t"
          "v_pk_add_f32 v[194:195], v[194:195], 1.0 op_sel_hi:[1,0]\n\t"
          "v_pk_add_f32 v[196:197], v[196:197], 1.0 op_sel_hi:[1,0]\n\t"
          "v_pk_add_f32 v[198:199], v[198:199], 1.0 op_sel_hi:[1,0]\n\t"
          "v_pk_add_f32 v[200:201], v[200:201], 1.0 op_sel_hi:[1,0]\n\t"
          "s_waitcnt vmcnt(0) lgkmcnt(0)\n\t"
          "v_mfma_f32_32x32x16_bf16 v[130:145], v[182:185], v[72:75], v[130:145]\n\t"
          "v_rcp_f32 v194, v194\n\t"
          "v_rcp_f32 v195, v195\n\t"
          "v_rcp_f32 v196, v196\n\t"
          "v_rcp_f32 v197, v197\n\t"
          "v_rcp_f32 v198, v198\n\t"
          "v_rcp_f32 v199, v199\n\t"
          "v_rcp_f32 v200, v200\n\t"
          "v_rcp_f32 v201, v201\n\t"
          "v_and_or_b32 v210, v182, %[chi], v216\n\t"
          "s_mov_b32 exec_lo, 0\n\t"
          "s_mov_b32 exec_hi, %[mlast]\n\t"
          "ds_write2_b32 v213, v210, v183 offset1:1\n\t"
          "s_mov_b64 exec, -1\n\t"
          "v_pk_mul_f32 v[194:195], v[194:195], v[154:155]\n\t"
          "v_pk_mul_f32 v[196:197], v[196:197], v[156:157]\n\t"
          "v_pk_mul_f32 v[198:199], v[198:199], v[158:159]\n\t"
          "v_pk_mul_f32 v[200:201], v[200:201], v[160:161]\n\t"
          "v_cvt_pk_bf16_f32 v206, v194, v195\n\t"
          "v_cvt_pk_bf16_f32 v207, v196, v197\n\t"
          "v_cvt_pk_bf16_f32 v208, v198, v199\n\t"
          "v_cvt_pk_bf16_f32 v209, v200, v201\n\t"
          "s_branch LA1_%=\n\t"
          "LT1_%=:\n\t"
          "s_setprio 0\n\t"
          "s_cmp_eq_u32 %[rem], 1\n\t"
          "s_cbranch_scc1 LLAST_%=\n\t"
          "s_waitcnt vmcnt(1) lgkmcnt(0)\n\t"
          "ds_read_b128 v[162:165], v124\n\t"
          "ds_read_b128 v[166:169], v125\n\t"
          "ds_read_b128 v[170:173], v126\n\t"
          "ds_read_b128 v[174:177], v127\n\t"
          "s_mov_b32 exec_lo, 0\n\t"
          "ds_write2_b32 v213, v211, v211 offset1:1\n\t"
          "ds_read_b64 v[178:179], v112 offset:1024\n\t"
          "s_mov_b32 exec_lo, -1\n\t"
          "v_add_u32 v214, v212, v115\n\t"
          "v_xor_b32 v213, v212, v114\n\t"
          "ds_read_b64 v[180:181], v214\n\t"
          "v_mfma_f32_32x32x16_bf16 v[80:95], v[186:189], v[202:205], v[80:95]\n\t"
          "v_exp_f32 v194, v146\n\t"
          "v_exp_f32 v195, v147\n\t"
          "v_exp_f32 v196, v148\n\t"
          "v_exp_f32 v197, v149\n\t"
          "v_mfma_f32_32x32x16_bf16 v[80:95], v[190:193], v[206:209], v[80:95]\n\t"
          "v_exp_f32 v198, v150\n\t"
          "v_exp_f32 v199, v151\n\t"
          "v_exp_f32 v200, v152\n\t"
          "v_exp_f32 v201, v153\n\t"
          "s_waitcnt lgkmcnt(3)\n\t"
          "v_pk_add_f32 v[194:195], v[194:195], 1.0 op_sel_hi:[1,0]\n\t"
          "v_pk_add_f32 v[196:197], v[196:197], 1.0 op_sel_hi:[1,0]\n\t"
          "v_pk_add_f32 v[198:199], v[198:199], 1.0 op_sel_hi:[1,0]\n\t"
          "v_pk_add_f32 v[200:201], v[200:201], 1.0 op_sel_hi:[1,0]\n\t"
          "v_rcp_f32 v194, v194\n\t"
          "v_rcp_f32 v195, v195\n\t"
          "v_rcp_f32 v196, v196\n\t"
          "v_rcp_f32 v197, v197\n\t"
          "v_rcp_f32 v198, v198\n\t"
          "v_rcp_f32 v199, v199\n\t"
          "v_rcp_f32 v200, v200\n\t"
          "v_rcp_f32 v201, v201\n\t"
          "v_pk_mul_f32 v[194:195], v[194:195], v[146:147]\n\t"
          "v_pk_mul_f32 v[196:197], v[196:197], v[148:149]\n\t"
          "v_pk_mul_f32 v[198:199], v[198:199], v[150:151]\n\t"
          "v_pk_mul_f32 v[200:201], v[200:201], v[152:153]\n\t"
          "v_cvt_pk_bf16_f32 v202, v194, v195\n\t"
          "v_cvt_pk_bf16_f32 v203, v196, v197\n\t"
          "v_cvt_pk_bf16_f32 v204, v198, v199\n\t"
          "v_cvt_pk_bf16_f32 v205, v200, v201\n\t"
          "s_sub_u32 %[rem], %[rem], 1\n\t"
          "v_add_u32 v112, 0x400, v112\n\t"
          "v_add_u32 v113, 0x400, v113\n\t"
          "v_mfma_f32_32x32x16_bf16 v[130:145], v[162:165], v[64:67], 0\n\t"
          "v_exp_f32 v194, v154\n\t"
          "v_exp_f32 v195, v155\n\t"
          "v_exp_f32 v196, v156\n\t"
          "v_exp_f32 v197, v157\n\t"
          "v_mfma_f32_32x32x16_bf16 v[130:145], v[166:169], v[68:71], v[130:145]\n\t"
          "v_exp_f32 v198, v158\n\t"
          "v_exp_f32 v199, v159\n\t"
          "v_exp_f32 v200, v160\n\t"
          "v_exp_f32 v201, v161\n\t"
          "v_pk_add_f32 v[194:195], v[194:195], 1.0 op_sel_hi:[1,0]\n\t"
          "v_pk_add_f32 v[196:197], v[196:197], 1.0 op_sel_hi:[1,0]\n\t"
          "v_pk_add_f32 v[198:199], v[198:199], 1.0 op_sel_hi:[1,0]\n\t"
          "v_pk_add_f32 v[200:201], v[200:201], 1.0 op_sel_hi:[1,0]\n\t"
          "s_waitcnt vmcnt(0) lgkmcnt(0)\n\t"
          "v_mfma_f32_32x32x16_bf16 v[130:145], v[178:181], v[72:75], v[130:145]\n\t"
          "v_rcp_f32 v194, v194\n\t"
          "v_rcp_f32 v195, v195\n\t"
          "v_rcp_f32 v196, v196\n\t"
          "v_rcp_f32 v197, v197\n\t"
          "v_rcp_f32 v198, v198\n\t"
          "v_rcp_f32 v199, v199\n\t"
          "v_rcp_f32 v200, v200\n\t"
          "v_rcp_f32 v201, v201\n\t"
          "v_and_or_b32 v210, v178, %[chi], v216\n\t"
          "s_mov_b32 exec_lo, 0\n\t"
          "s_mov_b32 exec_hi, %[mlast]\n\t"
          "ds_write2_b32 v213, v210, v179 offset1:1\n\t"
          "s_mov_b64 exec, -1\n\t"
          "v_pk_mul_f32 v[194:195], v[194:195], v[154:155]\n\t"
          "v_pk_mul_f32 v[196:197], v[196:197], v[156:157]\n\t"
          "v_pk_mul_f32 v[198:199], v[198:199], v[158:159]\n\t"
          "v_pk_mul_f32 v[200:201], v[200:201], v[160:161]\n\t"
          "v_cvt_pk_bf16_f32 v206, v194, v195\n\t"
          "v_cvt_pk_bf16_f32 v207, v196, v197\n\t"
          "v_cvt_pk_bf16_f32 v208, v198, v199\n\t"
          "v_cvt_pk_bf16_f32 v209, v200, v201\n\t"
          "s_branch LA0_%=\n\t"
          "LLAST_%=:\n\t"
          "s_waitcnt lgkmcnt(0)\n\t"
          "s_mov_b32 exec_lo, 0\n\t"
          "ds_write2_b32 v213, v211, v211 offset1:1\n\t"
          "s_mov_b32 exec_lo, -1\n\t"
          "v_mfma_f32_32x32x16_bf16 v[80:95], v[186:189], v[202:205], v[80:95]\n\t"
          "v_exp_f32 v194, v146\n\t"
          "v_exp_f32 v195, v147\n\t"
          "v_exp_f32 v196, v148\n\t"
          "v_exp_f32 v197, v149\n\t"
          "v_mfma_f32_32x32x16_bf16 v[80:95], v[190:193], v[206:209], v[80:95]\n\t"
          "v_exp_f32 v198, v150\n\t"
          "v_exp_f32 v199, v151\n\t"
          "v_exp_f32 v200, v152\n\t"
          "v_exp_f32 v201, v153\n\t"
          "v_pk_add_f32 v[194:195], v[194:195], 1.0 op_sel_hi:[1,0]\n\t"
          "v_pk_add_f32 v[196:197], v[196:197], 1.0 op_sel_hi:[1,0]\n\t"
          "v_pk_add_f32 v[198:199], v[198:199], 1.0 op_sel_hi:[1,0]\n\t"
          "v_pk_add_f32 v[200:201], v[200:201], 1.0 op_sel_hi:[1,0]\n\t"
          "v_rcp_f32 v194, v194\n\t"
          "v_rcp_f32 v195, v195\n\t"
          "v_rcp_f32 v196, v196\n\t"
          "v_rcp_f32 v197, v197\n\t"
          "v_rcp_f32 v198, v198\n\t"
          "v_rcp_f32 v199, v199\n\t"
          "v_rcp_f32 v200, v200\n\t"
          "v_rcp_f32 v201, v201\n\t"
          "v_pk_mul_f32 v[194:195], v[194:195], v[146:147]\n\t"
          "v_pk_mul_f32 v[196:197], v[196:197], v[148:149]\n\t"
          "v_pk_mul_f32 v[198:199], v[198:199], v[150:151]\n\t"
          "v_pk_mul_f32 v[200:201], v[200:201], v[152:153]\n\t"
          "v_cvt_pk_bf16_f32 v202, v194, v195\n\t"
          "v_cvt_pk_bf16_f32 v203, v196, v197\n\t"
          "v_cvt_pk_bf16_f32 v204, v198, v199\n\t"
          "v_cvt_pk_bf16_f32 v205, v200, v201\n\t"
          "v_exp_f32 v194, v154\n\t"
          "v_exp_f32 v195, v155\n\t"
          "v_exp_f32 v196, v156\n\t"
          "v_exp_f32 v197, v157\n\t"
          "v_mfma_f32_32x32x16_bf16 v[96:111], v[186:189], v[202:205], v[96:111]\n\t"
          "v_exp_f32 v198, v158\n\t"
          "v_exp_f32 v199, v159\n\t"
          "v_exp_f32 v200, v160\n\t"
          "v_exp_f32 v201, v161\n\t"
          "v_pk_add_f32 v[194:195], v[194:195], 1.0 op_sel_hi:[1,0]\n\t"
          "v_pk_add_f32 v[196:197], v[196:197], 1.0 op_sel_hi:[1,0]\n\t"
          "v_pk_add_f32 v[198:199], v[198:199], 1.0 op_sel_hi:[1,0]\n\t"
          "v_pk_add_f32 v[200:201], v[200:201], 1.0 op_sel_hi:[1,0]\n\t"
          "v_rcp_f32 v194, v194\n\t"
          "v_rcp_f32 v195, v195\n\t"
          "v_rcp_f32 v196, v196\n\t"
          "v_rcp_f32 v197, v197\n\t"
          "v_rcp_f32 v198, v198\n\t"
          "v_rcp_f32 v199, v199\n\t"
          "v_rcp_f32 v200, v200\n\t"
          "v_rcp_f32 v201, v201\n\t"
          "v_pk_mul_f32 v[194:195], v[194:195], v[154:155]\n\t"
          "v_pk_mul_f32 v[196:197], v[196:197], v[156:157]\n\t"
          "v_pk_mul_f32 v[198:199], v[198:199], v[158:159]\n\t"
          "v_pk_mul_f32 v[200:201], v[200:201], v[160:161]\n\t"
          "v_cvt_pk_bf16_f32 v206, v194, v195\n\t"
          "v_cvt_pk_bf16_f32 v207, v196, v197\n\t"
          "v_cvt_pk_bf16_f32 v208, v198, v199\n\t"
          "v_cvt_pk_bf16_f32 v209, v200, v201\n\t"
          "s_nop 1\n\t"
          "v_mfma_f32_32x32x16_bf16 v[96:111], v[190:193], v[206:209], v[96:111]\n\t"
          "s_nop 15\n\t"
          : "={v[80:95]}"(y0), "={v[96:111]}"(y1), "+{v[112:127]}"(cv), "+{v[178:179]}"(q0), [rem] "+s"(rem), [mk] "+s"(mk), [tmp] "=&s"(tmp)
          : "{v[64:71]}"(id), "{v[72:79]}"(bw), "{v[128:129]}"(trv), [mlast] "s"(mlast), [stg0] "s"(stg), [srd] "s"(srd), [eb] "s"(ea),
            [chi] "s"(0xffff0000u)
          : "v130", "v131", "v132", "v133", "v134", "v135", "v136", "v137", "v138", "v139", "v140", "v141", "v142", "v143", "v144", "v145", "v146", "v147", "v148", "v149", "v150", "v151", "v152", "v153", "v154", "v155", "v156", "v157", "v158", "v159", "v160", "v161", "v162", "v163", "v164", "v165", "v166", "v167", "v168", "v169", "v170", "v171", "v172", "v173", "v174", "v175", "v176", "v177", "v180", "v181", "v182", "v183", "v184", "v185", "v186", "v187", "v188", "v189", "v190", "v191", "v192", "v193", "v194", "v195", "v196", "v197", "v198", "v199", "v200", "v201", "v202", "v203", "v204", "v205", "v206", "v207", "v208", "v209", "v210", "v211", "v212", "v213", "v214", "v215", "v216", "scc", "memory");
      {
        const float x0 = __uint_as_float(wx0), x1 = __uint_as_float(wx1);
        if (!slow) {
#pragma unroll
          for (int g = 0; g < 4; ++g) {
            *(LAS float*)(size_t)fa[g] = y0[4 * g];
            *(LAS float*)(size_t)(fa[g] + 128) = y1[4 * g];
          }
        } else {
#pragma unroll
          for (int g = 0; g < 4; ++g) {
            atomicAdd((float*)(lds + fa[g]), y0[4 * g]);
            atomicAdd((float*)(lds + fa[g] + 128), y1[4 * g]);
          }
        }
        float z[12], u[6];
#pragma unroll
        for (int j = 0; j < 12; ++j) z[j] = y0[4 * (j / 3) + j % 3 + 1] * x0 + y1[4 * (j / 3) + j % 3 + 1] * x1;
        asm volatile("" : "+v"(z[0]), "+v"(z[1]), "+v"(z[2]), "+v"(z[3]), "+v"(z[4]), "+v"(z[5]), "+v"(z[6]), "+v"(z[7]), "+v"(z[8]),
                     "+v"(z[9]), "+v"(z[10]), "+v"(z[11]));
#pragma unroll
        for (int j = 0; j < 6; ++j) {
          const auto s2 = __builtin_amdgcn_permlane16_swap(__float_as_uint(z[j]), __float_as_uint(z[j + 6]), false, false);
          u[j] = __uint_as_float(s2[0]) + __uint_as_float(s2[1]);
        }
        float a0, a1, a2, zz;
        asm volatile(
            "s_nop 1\n\t"
            "v_add_f32_dpp %0, %4, %4 row_ror:8 row_mask:0xf bank_mask:0x3\n\t"
            "v_add_f32_dpp %1, %6, %6 row_ror:8 row_mask:0xf bank_mask:0x3\n\t"
            "v_add_f32_dpp %2, %8, %8 row_ror:8 row_mask:0xf bank_mask:0x3\n\t"
            "v_add_f32_dpp %0, %5, %5 row_ror:8 row_mask:0xf bank_mask:0xc\n\t"
            "v_add_f32_dpp %1, %7, %7 row_ror:8 row_mask:0xf bank_mask:0xc\n\t"
            "v_add_f32_dpp %2, %9, %9 row_ror:8 row_mask:0xf bank_mask:0xc\n\t"
            "s_nop 0\n\t"
            "v_add_f32_dpp %3, %0, %0 row_half_mirror row_mask:0xf bank_mask:0x5\n\t"
            "v_add_f32_dpp %3, %1, %1 row_half_mirror row_mask:0xf bank_mask:0xa\n\t"
            "v_add_f32_dpp %2, %2, %2 row_half_mirror row_mask:0xf bank_mask:0xf\n\t"
            "s_nop 0\n\t"
            "v_add_f32_dpp %3, %3, %3 quad_perm:[2,3,0,1] row_mask:0xf bank_mask:0xf\n\t"
            "v_add_f32_dpp %2, %2, %2 quad_perm:[2,3,0,1] row_mask:0xf bank_mask:0xf\n\t"
            "v_cndmask_b32 %3, %3, %2, %10\n\t"
            "s_nop 1\n\t"
            "v_add_f32_dpp %3, %3, %3 quad_perm:[1,0,3,2] row_mask:0xf bank_mask:0xf"
            : "=&v"(a0), "=&v"(a1), "=&v"(a2), "=&v"(zz)
            : "v"(u[0]), "v"(u[1]), "v"(u[2]), "v"(u[3]), "v"(u[4]), "v"(u[5]), "s"(0xccccccccccccccccull));
        if (!slow) *(LAS float*)(size_t)fz = zz; else atomicAdd((float*)(lds + fz), zz);
      }
      if (cp < ep) aim();
    }
    R0 += wcap;
    if (R0 >= nmax) break;
    lds_barrier();
    if (w == 0) *(u32*)(lds + L_CNT + lane * 4) = 0;
    lds_barrier();
    make_list();
    load_recs();
    load_pos();
    asm volatile("s_waitcnt vmcnt(0)" : "+v"(rec[0]), "+v"(rec[1]), "+v"(rec[2]), "+v"(rec[3]), "+v"(rec[4]), "+v"(rec[5]), "+v"(rec[6])::"memory");
  }

  lds_barrier();
  LANE_IDS;
  if (w < 6) {
    const int nl = (w >> 1) * 16 + i16;
    const char* mp = lds + L_MACC + min(nl, nn - 1) * 272 + q * 32;
    u32x4 bf[2];
    f32x4 m[8];
#pragma unroll
    for (int k = 0; k < 8; ++k) m[k] = *(const f32x4*)(mp + (k >> 2) * 128 + (k & 2) * 8 + (k & 1) * (40 * 272));
    asm volatile("" : "+v"(m[0]), "+v"(m[1]), "+v"(m[2]), "+v"(m[3]), "+v"(m[4]), "+v"(m[5]), "+v"(m[6]), "+v"(m[7]));
#pragma unroll
    for (int k = 0; k < 2; ++k) {
      const f32x4 m0 = (m[4 * k] + m[4 * k + 1]) * NLN2, m1 = (m[4 * k + 2] + m[4 * k + 3]) * NLN2;
      bf[k] = u32x4{pk_bf16(m0.x, m0.y), pk_bf16(m0.z, m0.w), pk_bf16(m1.x, m1.y), pk_bf16(m1.z, m1.w)};
    }
    asm volatile("s_nop 1" : "+v"(bf[0]), "+v"(bf[1]));
    u32x2 ov[2];
#pragma unroll
    for (int ct = 0; ct < 2; ++ct) {
      const f32x4 acc = MFMA16(wm[2 * ct + 1], bf[1], MFMA16(wm[2 * ct], bf[0], hacc[ct]));
      float o[4];
#pragma unroll
      for (int j = 0; j < 4; ++j) o[j] = acc[j] * __builtin_amdgcn_rcpf(1.f + __builtin_amdgcn_exp2f(acc[j] * NLOG2E));
      ov[ct] = u32x2{pk_bf16(bf_lo(hres[ct].x) + o[0], bf_hi(hres[ct].x) + o[1]), pk_bf16(bf_lo(hres[ct].y) + o[2], bf_hi(hres[ct].y) + o[3])};
    }
    asm volatile("s_nop 1" : "+v"(ov[0]), "+v"(ov[1]));
    const auto x = __builtin_amdgcn_permlane16_swap(ov[0].x, ov[1].x, false, false);
    const auto y = __builtin_amdgcn_permlane16_swap(ov[0].y, ov[1].y, false, false);
    const u32x4 row = {x[0], y[0], x[1], y[1]};
    const u32 oo = (nb0 + nl) * 128 + (w & 1) * 64 + (q & 1) * 32 + (q >> 1) * 16;
    if (nl < nn) asm volatile("global_store_dwordx4 %0, %1, %2 sc0 sc1\n\ts_nop 1" ::"v"(oo), "v"(row), "s"(out) : "memory");
  } else {
    const int i = tid - 384;
    if (i < nn * 3) {
      const int n = i / 3, k = i - 3 * n;
      const float pv = *(const float*)(lds + L_T + n * 16 + k * 4);
      const float z = *(const float*)(lds + L_ZACC + n * 16 + (k + 1) * 4) + *(const float*)(lds + L_ZACC + (40 + n) * 16 + (k + 1) * 4);
      asm volatile("global_store_short %0, %1, %2 sc0 sc1" ::"v"((u32)(NN * 64 + nb0 * 3 + i) * 2), "v"(pk_bf16(pv + NLN2 * z, 0.f)), "s"(out)
                   : "memory");
    }
  }
}

extern "C" void kernel_launch(void* const* d_in, const int* in_sizes, int n_in, void* d_out, int out_size, void* d_ws,
                              size_t ws_size, hipStream_t stream) {
  if (n_in != 8 || in_sizes[0] != NN * 64 || in_sizes[3] != NB * CH || out_size != NN * 67 || ws_size < WS_BT + (65536u << 7)) return;
  (void)hipFuncSetAttribute((const void*)k_egnn, hipFuncAttributeMaxDynamicSharedMemorySize, L_END);
  k_egnn<<<NB, 512, L_END, stream>>>((const u16*)d_in[0], (const u16*)d_in[1], (const u16*)d_in[2], (const int*)d_in[3],
                                     (const int*)d_in[4], (const u16*)d_in[5], (const u16*)d_in[6], (const u16*)d_in[7], (char*)d_ws,
                                     (u16*)d_out);
}
```
